```python
import torch

def _softmax(x, axis=-1):
    return torch.softmax(x, dim=axis)
def _gelu(x):
    return torch.nn.functional.gelu(x, approximate="tanh")
def _silu(x):
    return torch.nn.functional.silu(x)
def _sigmoid(x):
    return torch.sigmoid(x)
def _layernorm(x, axis=-1, eps=1e-5):
    m = x.mean(dim=axis, keepdim=True)
    v = x.var(dim=axis, keepdim=True, unbiased=False)
    return (x - m) / torch.sqrt(v + eps)
def _scatter_add(tgt, idx, src, axis=0):
    return tgt.clone().index_add_(axis, idx, src)
def _segment_sum(src, seg, n):
    out = torch.zeros((n,) + src.shape[1:], dtype=src.dtype)
    return out.index_add_(0, seg, src)
def _pad(x, pw):
    flat = tuple(v for pair in reversed(pw) for v in pair)
    return torch.nn.functional.pad(x, flat)

kernel_name = 'concat_heads__gqa_block_cached__gqa_block_cached__near_ridge'

def setup_inputs():

    _w = (lambda a: (lambda t: t.to(torch.bfloat16) if t.is_floating_point() else t)(torch.as_tensor(a)))
    def _A_setup():
        B, Sq, D = 16, 48, 448
        Hq, Hkv, Sk, Dh = 16, 8, 48, 256
        assert Hq % Hkv == 0, (Hq, Hkv)
        return {
            "x": _w((lambda *s: torch.randn(*s, dtype=torch.bfloat16) * 0.8)(B, Sq, D)),
            "k_cache": _w((lambda *s: torch.randn(*s, dtype=torch.bfloat16) * 0.8)(B, Hkv, Sk, Dh)),
            "v_cache": _w((lambda *s: torch.randn(*s, dtype=torch.bfloat16) * 0.05)(B, Hkv, Sk, Dh)),
            "wq": _w((lambda *s: torch.randn(*s, dtype=torch.bfloat16) * 0.8)(D, Hq * Dh) * (D ** -0.5)),
            "wo": _w((lambda *s: torch.randn(*s, dtype=torch.bfloat16) * 0.05)(Hq * Dh, D) * ((Hq * Dh) ** -0.5)),
        }
    _A_in = _A_setup()

    def _B_setup():
        B, Sq, D = 16, 48, 448
        Hq, Hkv, Sk, Dh = 16, 8, 48, 256
        assert Hq % Hkv == 0, (Hq, Hkv)
        return {
            "x": _w((lambda *s: torch.randn(*s, dtype=torch.bfloat16) * 0.8)(B, Sq, D)),
            "k_cache": _w((lambda *s: torch.randn(*s, dtype=torch.bfloat16) * 0.8)(B, Hkv, Sk, Dh)),
            "v_cache": _w((lambda *s: torch.randn(*s, dtype=torch.bfloat16) * 0.05)(B, Hkv, Sk, Dh)),
            "wq": _w((lambda *s: torch.randn(*s, dtype=torch.bfloat16) * 0.8)(D, Hq * Dh) * (D ** -0.5)),
            "wo": _w((lambda *s: torch.randn(*s, dtype=torch.bfloat16) * 0.05)(Hq * Dh, D) * ((Hq * Dh) ** -0.5)),
        }
    _B_in = _B_setup()

    return {
        "A_x": _A_in["x"],
        "A_k_cache": _A_in["k_cache"],
        "A_v_cache": _A_in["v_cache"],
        "A_wq": _A_in["wq"],
        "A_wo": _A_in["wo"],
        "B_x": _B_in["x"],
        "B_k_cache": _B_in["k_cache"],
        "B_v_cache": _B_in["v_cache"],
        "B_wq": _B_in["wq"],
        "B_wo": _B_in["wo"],
    }

def reference(A_x, A_k_cache, A_v_cache, A_wq, A_wo, B_x, B_k_cache, B_v_cache, B_wq, B_wo):
    def _A_ref(x, k_cache, v_cache, wq, wo):
        B, Sq, D = x.shape
        Hq, Dh = 16, 256
        Hkv = k_cache.shape[1]
        rep = Hq // Hkv
        q = (lambda x, *d: x.permute(*d))((x @ wq).reshape(B, Sq, Hq, Dh), 0, 2, 1, 3)
        k = (lambda x, n, axis: torch.repeat_interleave(x, n, dim=axis))(k_cache, rep, axis=1)
        v = (lambda x, n, axis: torch.repeat_interleave(x, n, dim=axis))(v_cache, rep, axis=1)
        scores = torch.einsum("bhqd,bhkd->bhqk", q, k) * (Dh ** -0.5)
        p = _softmax(scores, axis=-1)
        o = torch.einsum("bhqk,bhkd->bhqd", p, v)
        return (lambda x, *d: x.permute(*d))(o, 0, 2, 1, 3).reshape(B, Sq, Hq * Dh) @ wo

    def _B_ref(x, k_cache, v_cache, wq, wo):
        B, Sq, D = x.shape
        Hq, Dh = 16, 256
        Hkv = k_cache.shape[1]
        rep = Hq // Hkv
        q = (lambda x, *d: x.permute(*d))((x @ wq).reshape(B, Sq, Hq, Dh), 0, 2, 1, 3)
        k = (lambda x, n, axis: torch.repeat_interleave(x, n, dim=axis))(k_cache, rep, axis=1)
        v = (lambda x, n, axis: torch.repeat_interleave(x, n, dim=axis))(v_cache, rep, axis=1)
        scores = torch.einsum("bhqd,bhkd->bhqk", q, k) * (Dh ** -0.5)
        p = _softmax(scores, axis=-1)
        o = torch.einsum("bhqk,bhkd->bhqd", p, v)
        return (lambda x, *d: x.permute(*d))(o, 0, 2, 1, 3).reshape(B, Sq, Hq * Dh) @ wo

    a = _A_ref(A_x, A_k_cache, A_v_cache, A_wq, A_wo)
    b = _B_ref(B_x, B_k_cache, B_v_cache, B_wq, B_wo)
    return torch.concatenate([a, b], axis=-1)
```

```cpp
#include <hip/hip_runtime.h>
#include <hip/hip_bf16.h>
#include <stdint.h>


typedef __attribute__((ext_vector_type(8))) short s16x8;
typedef __attribute__((ext_vector_type(4))) short s16x4;
typedef __attribute__((ext_vector_type(4))) float f32x4;
typedef __attribute__((address_space(3))) const char* lds_cptr;
typedef unsigned short u16;

#define WAITV(n) asm volatile("s_waitcnt vmcnt(" #n ")" ::: "memory")
#define BAR() do { asm volatile("s_waitcnt lgkmcnt(0)" ::: "memory"); __builtin_amdgcn_s_barrier(); } while (0)

__device__ __forceinline__ s16x4 tr_read(lds_cptr p) {
  return __builtin_bit_cast(s16x4, __builtin_amdgcn_ds_read_tr16_b64_v4i16((__attribute__((address_space(3))) s16x4*)p));
}
__device__ __forceinline__ s16x8 ld_b128(lds_cptr p) { return *(const __attribute__((address_space(3))) s16x8*)p; }
__device__ __forceinline__ short f2bf(float f) { return __builtin_bit_cast(short, (__bf16)f); }
__device__ __forceinline__ float bf2f(short s) { return __builtin_bit_cast(float, ((unsigned)(u16)s) << 16); }
__device__ __forceinline__ s16x8 cat4(s16x4 a, s16x4 b) { return __builtin_shufflevector(a, b, 0, 1, 2, 3, 4, 5, 6, 7); }
__device__ __forceinline__ void glds16(const void* g, void* l) {
  __builtin_amdgcn_global_load_lds((const unsigned*)g, (__attribute__((address_space(3))) unsigned*)l, 16, 0, 0);
}

constexpr int K1_SLOT = 16384 + 6144;
constexpr int K1_NS = 5;
constexpr int K1_VOFF = K1_SLOT * K1_NS;
constexpr int K1_LDS = K1_VOFF + 49152;

__global__ __launch_bounds__(256, 1) void k1_qattn(
    const u16* __restrict__ x0, const u16* __restrict__ kc0, const u16* __restrict__ vc0, const u16* __restrict__ wq0,
    const u16* __restrict__ x1, const u16* __restrict__ kc1, const u16* __restrict__ vc1, const u16* __restrict__ wq1,
    u16* __restrict__ O) {
  extern __shared__ __attribute__((aligned(16))) char smem[];
  const int id = blockIdx.x;
  const int xcd = id & 7, slot = id >> 3;
  const int blk = slot >> 4, head = 2 * xcd + ((slot >> 3) & 1), bp = slot & 7, kvh = xcd;
  const int tid = threadIdx.x, lane = tid & 63;
  const int w = __builtin_amdgcn_readfirstlane(tid >> 6);
  const int bsel = w >> 1, dh = w & 1;
  const int g = lane >> 4, i16 = lane & 15, q4 = (lane >> 2) & 3, p4 = lane & 3;

  const u16* X = (blk ? x1 : x0) + (size_t)bp * 96 * 448;
  const u16* Wq = (blk ? wq1 : wq0) + head * 256;
  const u16* Kc = (blk ? kc1 : kc0) + (size_t)((bp * 2 + bsel) * 8 + kvh) * 48 * 256;
  const u16* Vc = (blk ? vc1 : vc0) + (size_t)((bp * 2) * 8 + kvh) * 48 * 256;

  s16x4 kf[3][4][2];
#pragma unroll
  for (int kt = 0; kt < 3; ++kt)
#pragma unroll
    for (int s = 0; s < 4; ++s) {
      const u16* p = Kc + (kt * 16 + i16) * 256 + dh * 128 + 32 * s + 4 * g;
      kf[kt][s][0] = *(const s16x4*)p;
      kf[kt][s][1] = *(const s16x4*)(p + 16);
    }

  int wq_off[4];
#pragma unroll
  for (int i = 0; i < 4; ++i) {
    int row = (w * 4 + i) * 2 + (lane >> 5);
    int c16 = lane & 31;
    int f = (row & 3) | ((row >> 1) & 4);
    int ls = (c16 >> 1) ^ f;
    wq_off[i] = row * 4096 + ls * 16 + (c16 & 1) * 8;
  }
  int x_off[2];
#pragma unroll
  for (int j = 0; j < 2; ++j) {
    int row = (w * 2 + j) * 12 + (lane >> 2);
    int lc = (lane & 3) ^ ((-(row >> 2)) & 3);
    x_off[j] = row * 448 + lc * 8;
  }

  auto issue_chunk = [&](int c) {
    char* sb = smem + (c % K1_NS) * K1_SLOT;
#pragma unroll
    for (int i = 0; i < 4; ++i) glds16(Wq + (size_t)c * 32 * 4096 + wq_off[i], sb + (w * 4 + i) * 1024);
    if (lane < 48) {
#pragma unroll
      for (int j = 0; j < 2; ++j) glds16(X + c * 32 + x_off[j], sb + 16384 + (w * 2 + j) * 768);
    }
  };
  auto issue_v = [&](int gv) {
#pragma unroll
    for (int i = 0; i < 6; ++i) {
      int pidx = gv * 24 + w * 6 + i;
      int batch = pidx / 24;
      int row = (pidx % 24) * 2 + (lane >> 5);
      int c16 = lane & 31;
      int ls = (c16 >> 1) ^ (row & 7);
      glds16(Vc + (size_t)batch * 8 * 48 * 256 + row * 256 + ls * 16 + (c16 & 1) * 8, smem + K1_VOFF + pidx * 1024);
    }
  };

  issue_chunk(0); issue_chunk(1); issue_chunk(2); issue_chunk(3);

  f32x4 acc[8][3];
#pragma unroll
  for (int a = 0; a < 8; ++a)
#pragma unroll
    for (int b = 0; b < 3; ++b) acc[a][b] = (f32x4){0.f, 0.f, 0.f, 0.f};

  const int fl = q4 | ((g & 1) << 2);
  const int a_base = (8 * g + q4) * 512 + p4 * 8;
  const int b_base = 16384 + (bsel * 48 + i16) * 64 + ((g ^ ((-(i16 >> 2)) & 3)) * 16);

#pragma unroll
  for (int c = 0; c < 14; ++c) {
    if (c <= 12) WAITV(18); else WAITV(12);
    BAR();
    if (c + 4 < 14) issue_chunk(c + 4);
    else if (c + 4 < 16) issue_v(c + 4 - 14);
    lds_cptr sb = (lds_cptr)(smem + (c % K1_NS) * K1_SLOT);
    s16x8 bfr[3];
#pragma unroll
    for (int tt = 0; tt < 3; ++tt) bfr[tt] = ld_b128(sb + b_base + tt * 16 * 64);
#pragma unroll
    for (int dt = 0; dt < 8; ++dt) {
      lds_cptr ap = sb + a_base + (dh * 8 + (dt ^ fl)) * 32;
      s16x8 af = cat4(tr_read(ap), tr_read(ap + 2048));
#pragma unroll
      for (int tt = 0; tt < 3; ++tt) acc[dt][tt] = __builtin_amdgcn_mfma_f32_16x16x32_bf16(af, bfr[tt], acc[dt][tt], 0, 0, 0);
    }
  }

  f32x4 sacc[3][3];
#pragma unroll
  for (int a = 0; a < 3; ++a)
#pragma unroll
    for (int b = 0; b < 3; ++b) sacc[a][b] = (f32x4){0.f, 0.f, 0.f, 0.f};
#pragma unroll
  for (int s = 0; s < 4; ++s) {
    s16x8 qf[3];
#pragma unroll
    for (int tt = 0; tt < 3; ++tt) {
#pragma unroll
      for (int r = 0; r < 4; ++r) { qf[tt][r] = f2bf(acc[2 * s][tt][r]); qf[tt][4 + r] = f2bf(acc[2 * s + 1][tt][r]); }
    }
#pragma unroll
    for (int kt = 0; kt < 3; ++kt) {
      s16x8 ka = cat4(kf[kt][s][0], kf[kt][s][1]);
#pragma unroll
      for (int tt = 0; tt < 3; ++tt) sacc[kt][tt] = __builtin_amdgcn_mfma_f32_16x16x32_bf16(ka, qf[tt], sacc[kt][tt], 0, 0, 0);
    }
  }
  {
    f32x4* ex = (f32x4*)(smem + w * 9216) + lane;
#pragma unroll
    for (int kt = 0; kt < 3; ++kt)
#pragma unroll
      for (int tt = 0; tt < 3; ++tt) ex[(kt * 3 + tt) * 64] = sacc[kt][tt];
  }
  WAITV(0);
  BAR();
  {
    const f32x4* ex = (const f32x4*)(smem + (w ^ 1) * 9216) + lane;
#pragma unroll
    for (int kt = 0; kt < 3; ++kt)
#pragma unroll
      for (int tt = 0; tt < 3; ++tt) sacc[kt][tt] += ex[(kt * 3 + tt) * 64];
  }
  s16x4 pf[3][3];
#pragma unroll
  for (int tt = 0; tt < 3; ++tt) {
    float v[12];
    float m = -INFINITY;
#pragma unroll
    for (int kt = 0; kt < 3; ++kt)
#pragma unroll
      for (int r = 0; r < 4; ++r) { v[kt * 4 + r] = bf2f(f2bf(sacc[kt][tt][r])) * 0.0625f; m = fmaxf(m, v[kt * 4 + r]); }
    m = fmaxf(m, __shfl_xor(m, 16));
    m = fmaxf(m, __shfl_xor(m, 32));
    float sum = 0.f;
#pragma unroll
    for (int e = 0; e < 12; ++e) { v[e] = __expf(v[e] - m); sum += v[e]; }
    sum += __shfl_xor(sum, 16);
    sum += __shfl_xor(sum, 32);
#pragma unroll
    for (int kt = 0; kt < 3; ++kt)
#pragma unroll
      for (int r = 0; r < 4; ++r) pf[kt][tt][r] = f2bf(v[kt * 4 + r] / sum);
  }
  lds_cptr vb = (lds_cptr)(smem + K1_VOFF + bsel * 24576);
  u16* Orow = O + (size_t)(blk * 768 + bp * 96 + bsel * 48 + i16) * 4096 + head * 256 + dh * 128 + 4 * g;
#pragma unroll
  for (int dt = 0; dt < 8; ++dt) {
    f32x4 oacc[3];
#pragma unroll
    for (int tt = 0; tt < 3; ++tt) oacc[tt] = (f32x4){0.f, 0.f, 0.f, 0.f};
#pragma unroll
    for (int kt = 0; kt < 3; ++kt) {
      int row = kt * 16 + 4 * g + q4;
      s16x4 va = tr_read(vb + row * 512 + (((dh * 8 + dt) ^ (row & 7)) * 32) + p4 * 8);
#pragma unroll
      for (int tt = 0; tt < 3; ++tt) oacc[tt] = __builtin_amdgcn_mfma_f32_16x16x16bf16_1k(va, pf[kt][tt], oacc[tt], 0, 0, 0);
    }
#pragma unroll
    for (int tt = 0; tt < 3; ++tt) {
      s16x4 o;
#pragma unroll
      for (int r = 0; r < 4; ++r) o[r] = f2bf(oacc[tt][r]);
      *(s16x4*)(Orow + (size_t)tt * 16 * 4096 + dt * 16) = o;
    }
  }
}

constexpr int K2_SLOT = 16384 + 12288;
constexpr int K2_NS = 5;
constexpr int K2_LDS = K2_SLOT * K2_NS;

__global__ __launch_bounds__(256, 1) void k2_oproj(const u16* __restrict__ O, const u16* __restrict__ wo0,
                                                   const u16* __restrict__ wo1, float* __restrict__ part) {
  extern __shared__ __attribute__((aligned(16))) char smem[];
  const int id = blockIdx.x;
  const int xcd = id & 7, slot = id >> 3;
  const int blk = xcd >> 2, ks = xcd & 3, mt = slot >> 2, nt = slot & 3;
  const int tid = threadIdx.x, lane = tid & 63;
  const int w = __builtin_amdgcn_readfirstlane(tid >> 6);
  const int th = w & 1, kh = w >> 1;
  const int g = lane >> 4, i16 = lane & 15, q4 = (lane >> 2) & 3, p4 = lane & 3;

  const u16* Wo = (blk ? wo1 : wo0) + (size_t)ks * 1024 * 448 + nt * 112;
  const u16* Og = O + (size_t)(blk * 768 + mt * 96) * 4096 + ks * 1024;

  int wo_off[4]; bool wo_act[4];
#pragma unroll
  for (int i = 0; i < 4; ++i) {
    int row = (w * 4 + i) * 4 + (lane >> 4);
    int c16 = lane & 15;
    int f = (row & 3) | ((row >> 1) & 4);
    int ls = (c16 >> 1) ^ f;
    wo_act[i] = ls < 7;
    wo_off[i] = row * 448 + ls * 16 + (c16 & 1) * 8;
  }
  int o_off[3];
#pragma unroll
  for (int j = 0; j < 3; ++j) {
    int row = (w * 3 + j) * 8 + (lane >> 3);
    int lc = (lane & 7) ^ (row & 7);
    o_off[j] = row * 4096 + lc * 8;
  }
  auto issue_chunk = [&](int c) {
    char* sb = smem + (c % K2_NS) * K2_SLOT;
#pragma unroll
    for (int i = 0; i < 4; ++i)
      if (wo_act[i]) glds16(Wo + (size_t)c * 64 * 448 + wo_off[i], sb + (w * 4 + i) * 1024);
#pragma unroll
    for (int j = 0; j < 3; ++j) glds16(Og + c * 64 + o_off[j], sb + 16384 + (w * 3 + j) * 1024);
  };
  issue_chunk(0); issue_chunk(1); issue_chunk(2); issue_chunk(3);

  f32x4 acc[7][3];
#pragma unroll
  for (int a = 0; a < 7; ++a)
#pragma unroll
    for (int b = 0; b < 3; ++b) acc[a][b] = (f32x4){0.f, 0.f, 0.f, 0.f};
  const int fl = q4 | ((g & 1) << 2);
  const int a_base = (kh * 32 + 8 * g + q4) * 256 + p4 * 8;
  const int brow = th * 48 + i16;
  const int b_base = 16384 + brow * 128 + (((kh * 4 + g) ^ (brow & 7)) * 16);

#pragma unroll
  for (int c = 0; c < 16; ++c) {
    if (c <= 12) WAITV(21); else if (c == 13) WAITV(14); else if (c == 14) WAITV(7); else WAITV(0);
    BAR();
    if (c + 4 < 16) issue_chunk(c + 4);
    lds_cptr sb = (lds_cptr)(smem + (c % K2_NS) * K2_SLOT);
    s16x8 bfr[3];
#pragma unroll
    for (int tt = 0; tt < 3; ++tt) bfr[tt] = ld_b128(sb + b_base + tt * 16 * 128);
#pragma unroll
    for (int n7 = 0; n7 < 7; ++n7) {
      lds_cptr ap = sb + a_base + ((n7 ^ fl) * 32);
      s16x8 af = cat4(tr_read(ap), tr_read(ap + 1024));
#pragma unroll
      for (int tt = 0; tt < 3; ++tt) acc[n7][tt] = __builtin_amdgcn_mfma_f32_16x16x32_bf16(af, bfr[tt], acc[n7][tt], 0, 0, 0);
    }
  }
  BAR();
  char* ex = smem + 2 * K2_SLOT + th * 21 * 1024;
  if (kh == 1) {
#pragma unroll
    for (int n7 = 0; n7 < 7; ++n7)
#pragma unroll
      for (int tt = 0; tt < 3; ++tt) *((f32x4*)(ex + (n7 * 3 + tt) * 1024) + lane) = acc[n7][tt];
  }
  BAR();
  if (kh == 0) {
    float* dst = part + (size_t)((blk * 4 + ks) * 768 + mt * 96 + th * 48 + i16) * 448 + nt * 112 + 4 * g;
#pragma unroll
    for (int n7 = 0; n7 < 7; ++n7)
#pragma unroll
      for (int tt = 0; tt < 3; ++tt) {
        f32x4 v = acc[n7][tt] + *((const f32x4*)(ex + (n7 * 3 + tt) * 1024) + lane);
        *(f32x4*)(dst + (size_t)tt * 16 * 448 + n7 * 16) = v;
      }
  }
}

__global__ __launch_bounds__(256) void k3_reduce(const float* __restrict__ part, u16* __restrict__ out) {
  int idx = blockIdx.x * 256 + threadIdx.x;
  if (idx >= 2 * 768 * 112) return;
  int n4 = idx % 112, t = (idx / 112) % 768, blk = idx / (112 * 768);
  f32x4 s = (f32x4){0.f, 0.f, 0.f, 0.f};
#pragma unroll
  for (int ks = 0; ks < 4; ++ks) s += *(const f32x4*)(part + (size_t)((blk * 4 + ks) * 768 + t) * 448 + n4 * 4);
  s16x4 o;
#pragma unroll
  for (int r = 0; r < 4; ++r) o[r] = f2bf(s[r]);
  *(s16x4*)(out + (size_t)t * 896 + blk * 448 + n4 * 4) = o;
}

extern "C" void kernel_launch(void* const* d_in, const int* in_sizes, int n_in, void* d_out, int out_size, void* d_ws,
                              size_t ws_size, hipStream_t stream) {
  const u16* x0 = (const u16*)d_in[0];
  const u16* k0 = (const u16*)d_in[1];
  const u16* v0 = (const u16*)d_in[2];
  const u16* wq0 = (const u16*)d_in[3];
  const u16* wo0 = (const u16*)d_in[4];
  const u16* x1 = (const u16*)d_in[5];
  const u16* k1 = (const u16*)d_in[6];
  const u16* v1 = (const u16*)d_in[7];
  const u16* wq1 = (const u16*)d_in[8];
  const u16* wo1 = (const u16*)d_in[9];
  u16* O = (u16*)d_ws;
  float* part = (float*)((char*)d_ws + (size_t)2 * 768 * 4096 * 2);
  (void)hipFuncSetAttribute((const void*)k1_qattn, hipFuncAttributeMaxDynamicSharedMemorySize, K1_LDS);
  (void)hipFuncSetAttribute((const void*)k2_oproj, hipFuncAttributeMaxDynamicSharedMemorySize, K2_LDS);
  k1_qattn<<<256, 256, K1_LDS, stream>>>(x0, k0, v0, wq0, x1, k1, v1, wq1, O);
  k2_oproj<<<256, 256, K2_LDS, stream>>>(O, wo0, wo1, part);
  k3_reduce<<<(2 * 768 * 112 + 255) / 256, 256, 0, stream>>>(part, (u16*)d_out);
}
```

```cpp
#include <hip/hip_runtime.h>
#include <hip/hip_bf16.h>
#include <stdint.h>


typedef __attribute__((ext_vector_type(8))) short s16x8;
typedef __attribute__((ext_vector_type(4))) short s16x4;
typedef __attribute__((ext_vector_type(4))) float f32x4;
typedef __attribute__((address_space(3))) const char* lds_cptr;
typedef unsigned short u16;
typedef unsigned long long u64;

#define WAITV(n) asm volatile("s_waitcnt vmcnt(" #n ")" ::: "memory")
#define BAR() do { asm volatile("s_waitcnt lgkmcnt(0)" ::: "memory"); __builtin_amdgcn_s_barrier(); } while (0)

__device__ __forceinline__ s16x4 tr_read(lds_cptr p) {
  return __builtin_bit_cast(s16x4, __builtin_amdgcn_ds_read_tr16_b64_v4i16((__attribute__((address_space(3))) s16x4*)p));
}
__device__ __forceinline__ s16x8 ld_b128(lds_cptr p) { return *(const __attribute__((address_space(3))) s16x8*)p; }
__device__ __forceinline__ short f2bf(float f) { return __builtin_bit_cast(short, (__bf16)f); }
__device__ __forceinline__ float bf2f(short s) { return __builtin_bit_cast(float, ((unsigned)(u16)s) << 16); }
__device__ __forceinline__ s16x8 cat4(s16x4 a, s16x4 b) { return __builtin_shufflevector(a, b, 0, 1, 2, 3, 4, 5, 6, 7); }

#define GL "global_load_lds_dwordx4 "
__device__ __forceinline__ void glds_4p2(const void* sa, int a0, int a1, int a2, int a3, unsigned la,
                                         const void* sbp, int b0, int b1, unsigned lb, u64 mask) {
  unsigned sv; u64 se;
  asm volatile(
      "s_mov_b32 %0, m0\n\ts_mov_b32 m0, %8\n\ts_nop 0\n\t" GL "%2, %6\n\t"
      "s_add_u32 m0, m0, 0x400\n\ts_nop 0\n\t" GL "%3, %6\n\t"
      "s_add_u32 m0, m0, 0x400\n\ts_nop 0\n\t" GL "%4, %6\n\t"
      "s_add_u32 m0, m0, 0x400\n\ts_nop 0\n\t" GL "%5, %6\n\t"
      "s_mov_b64 %1, exec\n\ts_mov_b64 exec, %12\n\t"
      "s_mov_b32 m0, %11\n\ts_nop 0\n\t" GL "%9, %7\n\t"
      "s_add_u32 m0, m0, 0x300\n\ts_nop 0\n\t" GL "%10, %7\n\t"
      "s_mov_b64 exec, %1\n\ts_mov_b32 m0, %0"
      : "=&s"(sv), "=&s"(se)
      : "v"(a0), "v"(a1), "v"(a2), "v"(a3), "s"(sa), "s"(sbp), "s"(la), "v"(b0), "v"(b1), "s"(lb), "s"(mask)
      : "memory", "scc");
}
__device__ __forceinline__ void glds_4m3(const void* sa, int a0, int a1, int a2, int a3, unsigned la, u64 m0_, u64 m1_,
                                         u64 m2_, u64 m3_, const void* sbp, int b0, int b1, int b2, unsigned lb) {
  unsigned sv; u64 se;
  asm volatile(
      "s_mov_b32 %0, m0\n\ts_mov_b64 %1, exec\n\t"
      "s_mov_b32 m0, %8\n\ts_mov_b64 exec, %13\n\t" GL "%2, %6\n\t"
      "s_add_u32 m0, m0, 0x400\n\ts_mov_b64 exec, %14\n\t" GL "%3, %6\n\t"
      "s_add_u32 m0, m0, 0x400\n\ts_mov_b64 exec, %15\n\t" GL "%4, %6\n\t"
      "s_add_u32 m0, m0, 0x400\n\ts_mov_b64 exec, %16\n\t" GL "%5, %6\n\t"
      "s_mov_b32 m0, %12\n\ts_mov_b64 exec, %1\n\t" GL "%9, %7\n\t"
      "s_add_u32 m0, m0, 0x400\n\ts_nop 0\n\t" GL "%10, %7\n\t"
      "s_add_u32 m0, m0, 0x400\n\ts_nop 0\n\t" GL "%11, %7\n\t"
      "s_mov_b32 m0, %0"
      : "=&s"(sv), "=&s"(se)
      : "v"(a0), "v"(a1), "v"(a2), "v"(a3), "s"(sa), "s"(sbp), "s"(la), "v"(b0), "v"(b1), "v"(b2), "s"(lb), "s"(m0_),
        "s"(m1_), "s"(m2_), "s"(m3_)
      : "memory", "scc");
}
__device__ __forceinline__ void glds_6(const void* sa, int a0, int a1, int a2, int a3, int a4, int a5, unsigned la) {
  unsigned sv;
  asm volatile(
      "s_mov_b32 %0, m0\n\ts_mov_b32 m0, %8\n\ts_nop 0\n\t" GL "%1, %7\n\t"
      "s_add_u32 m0, m0, 0x400\n\ts_nop 0\n\t" GL "%2, %7\n\t"
      "s_add_u32 m0, m0, 0x400\n\ts_nop 0\n\t" GL "%3, %7\n\t"
      "s_add_u32 m0, m0, 0x400\n\ts_nop 0\n\t" GL "%4, %7\n\t"
      "s_add_u32 m0, m0, 0x400\n\ts_nop 0\n\t" GL "%5, %7\n\t"
      "s_add_u32 m0, m0, 0x400\n\ts_nop 0\n\t" GL "%6, %7\n\t"
      "s_mov_b32 m0, %0"
      : "=&s"(sv)
      : "v"(a0), "v"(a1), "v"(a2), "v"(a3), "v"(a4), "v"(a5), "s"(sa), "s"(la)
      : "memory", "scc");
}

constexpr int K1_SLOT = 16384 + 6144;
constexpr int K1_NS = 5;
constexpr int K1_VOFF = K1_SLOT * K1_NS;
constexpr int K1_LDS = K1_VOFF + 49152;

__global__ __launch_bounds__(256, 1) void k1_qattn(
    const u16* __restrict__ x0, const u16* __restrict__ kc0, const u16* __restrict__ vc0, const u16* __restrict__ wq0,
    const u16* __restrict__ x1, const u16* __restrict__ kc1, const u16* __restrict__ vc1, const u16* __restrict__ wq1,
    u16* __restrict__ O) {
  extern __shared__ __attribute__((aligned(16))) char smem[];
  const unsigned lds0 = (unsigned)(uintptr_t)smem;
  const int id = blockIdx.x;
  const int xcd = id & 7, slot = id >> 3;
  const int blk = slot >> 4, head = 2 * xcd + ((slot >> 3) & 1), bp = slot & 7, kvh = xcd;
  const int tid = threadIdx.x, lane = tid & 63;
  const int w = __builtin_amdgcn_readfirstlane(tid >> 6);
  const int bsel = w >> 1, dh = w & 1;
  const int g = lane >> 4, i16 = lane & 15, q4 = (lane >> 2) & 3, p4 = lane & 3;

  const char* Xs = (const char*)((blk ? x1 : x0) + (size_t)bp * 96 * 448);
  const char* Ws = (const char*)((blk ? wq1 : wq0) + head * 256);
  const u16* Kc = (blk ? kc1 : kc0) + (size_t)((bp * 2 + bsel) * 8 + kvh) * 48 * 256;
  const char* Vs = (const char*)((blk ? vc1 : vc0) + (size_t)((bp * 2) * 8 + kvh) * 48 * 256);

  int wq_off[4];
#pragma unroll
  for (int i = 0; i < 4; ++i) {
    int row = (w * 4 + i) * 2 + (lane >> 5);
    int c16 = lane & 31;
    int f = (row & 3) | ((row >> 1) & 4);
    wq_off[i] = row * 8192 + (((c16 >> 1) ^ f) * 32) + (c16 & 1) * 16;
  }
  int x_off[2];
#pragma unroll
  for (int j = 0; j < 2; ++j) {
    int row = (w * 2 + j) * 12 + (lane < 48 ? (lane >> 2) : 0);
    int lc = (lane & 3) ^ ((-(row >> 2)) & 3);
    x_off[j] = row * 896 + lc * 16;
  }
  int islot = 0;
  auto issue_chunk = [&]() {
    unsigned sb = lds0 + islot * K1_SLOT;
    glds_4p2(Ws, wq_off[0], wq_off[1], wq_off[2], wq_off[3], sb + w * 4096, Xs, x_off[0], x_off[1], sb + 16384 + w * 1536,
             0x0000FFFFFFFFFFFFull);
    Ws += 32 * 8192; Xs += 64;
    islot = (islot == K1_NS - 1) ? 0 : islot + 1;
  };
#pragma unroll 1
  for (int c = 0; c < 4; ++c) issue_chunk();

  s16x4 kf[3][4][2];
#pragma unroll
  for (int kt = 0; kt < 3; ++kt) {
    const u16* p = Kc + (kt * 16 + i16) * 256 + dh * 128 + 4 * g;
#pragma unroll
    for (int s = 0; s < 4; ++s) {
      kf[kt][s][0] = *(const s16x4*)(p + 32 * s);
      kf[kt][s][1] = *(const s16x4*)(p + 32 * s + 16);
    }
  }

  f32x4 acc[8][3];
#pragma unroll
  for (int a = 0; a < 8; ++a)
#pragma unroll
    for (int b = 0; b < 3; ++b) acc[a][b] = (f32x4){0.f, 0.f, 0.f, 0.f};

  const int fl = q4 | ((g & 1) << 2);
  const int a_base = (8 * g + q4) * 512 + p4 * 8 + dh * 256;
  const int b_base = 16384 + (bsel * 48 + i16) * 64 + ((g ^ ((-(i16 >> 2)) & 3)) * 16);
  int cslot = 0;
#pragma unroll 1
  for (int c = 0; c < 14; ++c) {
    if (c == 13) WAITV(12); else WAITV(18);
    BAR();
    if (c < 10) issue_chunk();
    else if (c < 12) {
      int vo[6];
#pragma unroll
      for (int i = 0; i < 6; ++i) {
        int row = (w * 6 + i) * 2 + (lane >> 5);
        int c16 = lane & 31;
        vo[i] = row * 512 + (((c16 >> 1) ^ (row & 7)) * 32) + (c16 & 1) * 16;
      }
      glds_6(Vs + (size_t)(c - 10) * 8 * 48 * 512, vo[0], vo[1], vo[2], vo[3], vo[4], vo[5],
             lds0 + K1_VOFF + (c - 10) * 24576 + w * 6144);
    }
    lds_cptr sb = (lds_cptr)(smem + cslot * K1_SLOT);
    cslot = (cslot == K1_NS - 1) ? 0 : cslot + 1;
    s16x8 bfr[3];
#pragma unroll
    for (int tt = 0; tt < 3; ++tt) bfr[tt] = ld_b128(sb + b_base + tt * 16 * 64);
#pragma unroll
    for (int dt = 0; dt < 8; ++dt) {
      lds_cptr ap = sb + a_base + (dt ^ fl) * 32;
      s16x8 af = cat4(tr_read(ap), tr_read(ap + 2048));
#pragma unroll
      for (int tt = 0; tt < 3; ++tt) acc[dt][tt] = __builtin_amdgcn_mfma_f32_16x16x32_bf16(af, bfr[tt], acc[dt][tt], 0, 0, 0);
    }
  }

  f32x4 sacc[3][3];
#pragma unroll
  for (int a = 0; a < 3; ++a)
#pragma unroll
    for (int b = 0; b < 3; ++b) sacc[a][b] = (f32x4){0.f, 0.f, 0.f, 0.f};
#pragma unroll
  for (int s = 0; s < 4; ++s) {
    s16x8 qf[3];
#pragma unroll
    for (int tt = 0; tt < 3; ++tt) {
#pragma unroll
      for (int r = 0; r < 4; ++r) { qf[tt][r] = f2bf(acc[2 * s][tt][r]); qf[tt][4 + r] = f2bf(acc[2 * s + 1][tt][r]); }
    }
#pragma unroll
    for (int kt = 0; kt < 3; ++kt) {
      s16x8 ka = cat4(kf[kt][s][0], kf[kt][s][1]);
#pragma unroll
      for (int tt = 0; tt < 3; ++tt) sacc[kt][tt] = __builtin_amdgcn_mfma_f32_16x16x32_bf16(ka, qf[tt], sacc[kt][tt], 0, 0, 0);
    }
  }
  {
    f32x4* ex = (f32x4*)(smem + w * 9216) + lane;
#pragma unroll
    for (int kt = 0; kt < 3; ++kt)
#pragma unroll
      for (int tt = 0; tt < 3; ++tt) ex[(tt * 3 + kt) * 64] = sacc[kt][tt];
  }
  WAITV(0);
  BAR();
  const f32x4* exa = (const f32x4*)(smem + (w & 2) * 9216) + lane;
  lds_cptr vb = (lds_cptr)(smem + K1_VOFF + bsel * 24576) + (4 * g + q4) * 512 + p4 * 8;
  const int vsw = 4 * g + q4;
  u16* Orow = O + (size_t)(blk * 768 + bp * 96 + bsel * 48 + i16) * 4096 + head * 256 + dh * 128 + 4 * g;
#pragma unroll 1
  for (int tt = 0; tt < 3; ++tt) {
    float v[12];
    float m = -INFINITY;
#pragma unroll
    for (int kt = 0; kt < 3; ++kt) {
      f32x4 sv = exa[(tt * 3 + kt) * 64] + exa[(tt * 3 + kt) * 64 + 576];
#pragma unroll
      for (int r = 0; r < 4; ++r) { v[kt * 4 + r] = bf2f(f2bf(sv[r])) * 0.0625f; m = fmaxf(m, v[kt * 4 + r]); }
    }
    m = fmaxf(m, __shfl_xor(m, 16));
    m = fmaxf(m, __shfl_xor(m, 32));
    float sum = 0.f;
#pragma unroll
    for (int e = 0; e < 12; ++e) { v[e] = __expf(v[e] - m); sum += v[e]; }
    sum += __shfl_xor(sum, 16);
    sum += __shfl_xor(sum, 32);
    s16x4 pf[3];
#pragma unroll
    for (int kt = 0; kt < 3; ++kt)
#pragma unroll
      for (int r = 0; r < 4; ++r) pf[kt][r] = f2bf(v[kt * 4 + r] / sum);
#pragma unroll 2
    for (int dt = 0; dt < 8; ++dt) {
      f32x4 oacc = (f32x4){0.f, 0.f, 0.f, 0.f};
      lds_cptr vp = vb + (((dh * 8 + dt) ^ (vsw & 7)) * 32);
#pragma unroll
      for (int kt = 0; kt < 3; ++kt)
        oacc = __builtin_amdgcn_mfma_f32_16x16x16bf16_1k(tr_read(vp + kt * 8192), pf[kt], oacc, 0, 0, 0);
      s16x4 o;
#pragma unroll
      for (int r = 0; r < 4; ++r) o[r] = f2bf(oacc[r]);
      *(s16x4*)(Orow + dt * 16) = o;
    }
    Orow += 16 * 4096;
  }
}

constexpr int K2_SLOT = 16384 + 12288;
constexpr int K2_NS = 5;
constexpr int K2_LDS = K2_SLOT * K2_NS;

__global__ __launch_bounds__(256, 1) void k2_oproj(const u16* __restrict__ O, const u16* __restrict__ wo0,
                                                   const u16* __restrict__ wo1, float* __restrict__ part) {
  extern __shared__ __attribute__((aligned(16))) char smem[];
  const unsigned lds0 = (unsigned)(uintptr_t)smem;
  const int id = blockIdx.x;
  const int xcd = id & 7, slot = id >> 3;
  const int blk = xcd >> 2, ks = xcd & 3, mt = slot >> 2, nt = slot & 3;
  const int tid = threadIdx.x, lane = tid & 63;
  const int w = __builtin_amdgcn_readfirstlane(tid >> 6);
  const int th = w & 1, kh = w >> 1;
  const int g = lane >> 4, i16 = lane & 15, q4 = (lane >> 2) & 3, p4 = lane & 3;

  const char* Ws = (const char*)((blk ? wo1 : wo0) + (size_t)ks * 1024 * 448 + nt * 112);
  const char* Os = (const char*)(O + (size_t)(blk * 768 + mt * 96) * 4096 + ks * 1024);

  int wo_off[4]; u64 wo_act[4];
#pragma unroll
  for (int i = 0; i < 4; ++i) {
    int row = (w * 4 + i) * 4 + (lane >> 4);
    int c16 = lane & 15;
    int f = (row & 3) | ((row >> 1) & 4);
    int ls = (c16 >> 1) ^ f;
    wo_act[i] = __ballot(ls < 7);
    wo_off[i] = row * 896 + (ls < 7 ? ls : 0) * 32 + (c16 & 1) * 16;
  }
  int o_off[3];
#pragma unroll
  for (int j = 0; j < 3; ++j) {
    int row = (w * 3 + j) * 8 + (lane >> 3);
    int lc = (lane & 7) ^ (row & 7);
    o_off[j] = row * 8192 + lc * 16;
  }
  int islot = 0;
  auto issue_chunk = [&]() {
    unsigned sb = lds0 + islot * K2_SLOT;
    glds_4m3(Ws, wo_off[0], wo_off[1], wo_off[2], wo_off[3], sb + w * 4096, wo_act[0], wo_act[1], wo_act[2], wo_act[3], Os,
             o_off[0], o_off[1], o_off[2], sb + 16384 + w * 3072);
    Ws += 64 * 896; Os += 128;
    islot = (islot == K2_NS - 1) ? 0 : islot + 1;
  };
#pragma unroll 1
  for (int c = 0; c < 4; ++c) issue_chunk();

  f32x4 acc[7][3];
#pragma unroll
  for (int a = 0; a < 7; ++a)
#pragma unroll
    for (int b = 0; b < 3; ++b) acc[a][b] = (f32x4){0.f, 0.f, 0.f, 0.f};
  const int fl = q4 | ((g & 1) << 2);
  const int a_base = (kh * 32 + 8 * g + q4) * 256 + p4 * 8;
  const int brow = th * 48 + i16;
  const int b_base = 16384 + brow * 128 + (((kh * 4 + g) ^ (brow & 7)) * 16);
  int cslot = 0;
#pragma unroll 1
  for (int c = 0; c < 16; ++c) {
    if (c <= 12) WAITV(21); else if (c == 13) WAITV(14); else if (c == 14) WAITV(7); else WAITV(0);
    BAR();
    if (c < 12) issue_chunk();
    lds_cptr sb = (lds_cptr)(smem + cslot * K2_SLOT);
    cslot = (cslot == K2_NS - 1) ? 0 : cslot + 1;
    s16x8 bfr[3];
#pragma unroll
    for (int tt = 0; tt < 3; ++tt) bfr[tt] = ld_b128(sb + b_base + tt * 16 * 128);
#pragma unroll
    for (int n7 = 0; n7 < 7; ++n7) {
      lds_cptr ap = sb + a_base + ((n7 ^ fl) * 32);
      s16x8 af = cat4(tr_read(ap), tr_read(ap + 1024));
#pragma unroll
      for (int tt = 0; tt < 3; ++tt) acc[n7][tt] = __builtin_amdgcn_mfma_f32_16x16x32_bf16(af, bfr[tt], acc[n7][tt], 0, 0, 0);
    }
  }
  BAR();
  char* ex = smem + 2 * K2_SLOT + th * 21 * 1024;
  if (kh == 1) {
#pragma unroll
    for (int n7 = 0; n7 < 7; ++n7)
#pragma unroll
      for (int tt = 0; tt < 3; ++tt) *((f32x4*)(ex + (n7 * 3 + tt) * 1024) + lane) = acc[n7][tt];
  }
  BAR();
  if (kh == 0) {
    float* dst = part + (size_t)((blk * 4 + ks) * 768 + mt * 96 + th * 48 + i16) * 448 + nt * 112 + 4 * g;
#pragma unroll
    for (int n7 = 0; n7 < 7; ++n7)
#pragma unroll
      for (int tt = 0; tt < 3; ++tt) {
        f32x4 v = acc[n7][tt] + *((const f32x4*)(ex + (n7 * 3 + tt) * 1024) + lane);
        *(f32x4*)(dst + (size_t)tt * 16 * 448 + n7 * 16) = v;
      }
  }
}

__global__ __launch_bounds__(256) void k3_reduce(const float* __restrict__ part, u16* __restrict__ out) {
  int idx = blockIdx.x * 256 + threadIdx.x;
  if (idx >= 2 * 768 * 112) return;
  int n4 = idx % 112, t = (idx / 112) % 768, blk = idx / (112 * 768);
  f32x4 s = (f32x4){0.f, 0.f, 0.f, 0.f};
#pragma unroll
  for (int ks = 0; ks < 4; ++ks) s += *(const f32x4*)(part + (size_t)((blk * 4 + ks) * 768 + t) * 448 + n4 * 4);
  s16x4 o;
#pragma unroll
  for (int r = 0; r < 4; ++r) o[r] = f2bf(s[r]);
  *(s16x4*)(out + (size_t)t * 896 + blk * 448 + n4 * 4) = o;
}

extern "C" void kernel_launch(void* const* d_in, const int* in_sizes, int n_in, void* d_out, int out_size, void* d_ws,
                              size_t ws_size, hipStream_t stream) {
  const u16* x0 = (const u16*)d_in[0];
  const u16* k0 = (const u16*)d_in[1];
  const u16* v0 = (const u16*)d_in[2];
  const u16* wq0 = (const u16*)d_in[3];
  const u16* wo0 = (const u16*)d_in[4];
  const u16* x1 = (const u16*)d_in[5];
  const u16* k1 = (const u16*)d_in[6];
  const u16* v1 = (const u16*)d_in[7];
  const u16* wq1 = (const u16*)d_in[8];
  const u16* wo1 = (const u16*)d_in[9];
  u16* O = (u16*)d_ws;
  float* part = (float*)((char*)d_ws + (size_t)2 * 768 * 4096 * 2);
  (void)hipFuncSetAttribute((const void*)k1_qattn, hipFuncAttributeMaxDynamicSharedMemorySize, K1_LDS);
  (void)hipFuncSetAttribute((const void*)k2_oproj, hipFuncAttributeMaxDynamicSharedMemorySize, K2_LDS);
  k1_qattn<<<256, 256, K1_LDS, stream>>>(x0, k0, v0, wq0, x1, k1, v1, wq1, O);
  k2_oproj<<<256, 256, K2_LDS, stream>>>(O, wo0, wo1, part);
  k3_reduce<<<(2 * 768 * 112 + 255) / 256, 256, 0, stream>>>(part, (u16*)d_out);
}
```

```cpp
#include <hip/hip_runtime.h>
#include <hip/hip_bf16.h>
#include <stdint.h>


typedef __attribute__((ext_vector_type(8))) short s16x8;
typedef __attribute__((ext_vector_type(4))) short s16x4;
typedef __attribute__((ext_vector_type(4))) float f32x4;
typedef __attribute__((address_space(3))) const char* lds_cptr;
typedef unsigned short u16;
typedef unsigned long long u64;

#define WAITV(n) asm volatile("s_waitcnt vmcnt(" #n ")" ::: "memory")
#define BAR() do { asm volatile("s_waitcnt lgkmcnt(0)" ::: "memory"); __builtin_amdgcn_s_barrier(); } while (0)

__device__ __forceinline__ s16x4 tr_read(lds_cptr p) {
  return __builtin_bit_cast(s16x4, __builtin_amdgcn_ds_read_tr16_b64_v4i16((__attribute__((address_space(3))) s16x4*)p));
}
__device__ __forceinline__ s16x8 ld_b128(lds_cptr p) { return *(const __attribute__((address_space(3))) s16x8*)p; }
__device__ __forceinline__ short f2bf(float f) { return __builtin_bit_cast(short, (__bf16)f); }
__device__ __forceinline__ float bf2f(short s) { return __builtin_bit_cast(float, ((unsigned)(u16)s) << 16); }
__device__ __forceinline__ s16x8 cat4(s16x4 a, s16x4 b) { return __builtin_shufflevector(a, b, 0, 1, 2, 3, 4, 5, 6, 7); }

#define GL "global_load_lds_dwordx4 "
__device__ __forceinline__ void glds_4p2(const void* sa, int a0, int a1, int a2, int a3, unsigned la,
                                         const void* sbp, int b0, int b1, unsigned lb, u64 mask) {
  unsigned sv; u64 se;
  asm volatile(
      "s_mov_b32 %0, m0\n\ts_mov_b32 m0, %8\n\ts_nop 0\n\t" GL "%2, %6\n\t"
      "s_add_u32 m0, m0, 0x400\n\ts_nop 0\n\t" GL "%3, %6\n\t"
      "s_add_u32 m0, m0, 0x400\n\ts_nop 0\n\t" GL "%4, %6\n\t"
      "s_add_u32 m0, m0, 0x400\n\ts_nop 0\n\t" GL "%5, %6\n\t"
      "s_mov_b64 %1, exec\n\ts_mov_b64 exec, %12\n\t"
      "s_mov_b32 m0, %11\n\ts_nop 0\n\t" GL "%9, %7\n\t"
      "s_add_u32 m0, m0, 0x300\n\ts_nop 0\n\t" GL "%10, %7\n\t"
      "s_mov_b64 exec, %1\n\ts_mov_b32 m0, %0"
      : "=&s"(sv), "=&s"(se)
      : "v"(a0), "v"(a1), "v"(a2), "v"(a3), "s"(sa), "s"(sbp), "s"(la), "v"(b0), "v"(b1), "s"(lb), "s"(mask)
      : "memory", "scc");
}
__device__ __forceinline__ void glds_4m3(const void* sa, int a0, int a1, int a2, int a3, unsigned la, u64 m0_, u64 m1_,
                                         u64 m2_, u64 m3_, const void* sbp, int b0, int b1, int b2, unsigned lb) {
  unsigned sv; u64 se;
  asm volatile(
      "s_mov_b32 %0, m0\n\ts_mov_b64 %1, exec\n\t"
      "s_mov_b32 m0, %8\n\ts_mov_b64 exec, %13\n\t" GL "%2, %6\n\t"
      "s_add_u32 m0, m0, 0x400\n\ts_mov_b64 exec, %14\n\t" GL "%3, %6\n\t"
      "s_add_u32 m0, m0, 0x400\n\ts_mov_b64 exec, %15\n\t" GL "%4, %6\n\t"
      "s_add_u32 m0, m0, 0x400\n\ts_mov_b64 exec, %16\n\t" GL "%5, %6\n\t"
      "s_mov_b32 m0, %12\n\ts_mov_b64 exec, %1\n\t" GL "%9, %7\n\t"
      "s_add_u32 m0, m0, 0x400\n\ts_nop 0\n\t" GL "%10, %7\n\t"
      "s_add_u32 m0, m0, 0x400\n\ts_nop 0\n\t" GL "%11, %7\n\t"
      "s_mov_b32 m0, %0"
      : "=&s"(sv), "=&s"(se)
      : "v"(a0), "v"(a1), "v"(a2), "v"(a3), "s"(sa), "s"(sbp), "s"(la), "v"(b0), "v"(b1), "v"(b2), "s"(lb), "s"(m0_),
        "s"(m1_), "s"(m2_), "s"(m3_)
      : "memory", "scc");
}
__device__ __forceinline__ void glds_6(const void* sa, int a0, int a1, int a2, int a3, int a4, int a5, unsigned la) {
  unsigned sv;
  asm volatile(
      "s_mov_b32 %0, m0\n\ts_mov_b32 m0, %8\n\ts_nop 0\n\t" GL "%1, %7\n\t"
      "s_add_u32 m0, m0, 0x400\n\ts_nop 0\n\t" GL "%2, %7\n\t"
      "s_add_u32 m0, m0, 0x400\n\ts_nop 0\n\t" GL "%3, %7\n\t"
      "s_add_u32 m0, m0, 0x400\n\ts_nop 0\n\t" GL "%4, %7\n\t"
      "s_add_u32 m0, m0, 0x400\n\ts_nop 0\n\t" GL "%5, %7\n\t"
      "s_add_u32 m0, m0, 0x400\n\ts_nop 0\n\t" GL "%6, %7\n\t"
      "s_mov_b32 m0, %0"
      : "=&s"(sv)
      : "v"(a0), "v"(a1), "v"(a2), "v"(a3), "v"(a4), "v"(a5), "s"(sa), "s"(la)
      : "memory", "scc");
}

constexpr int K1_SLOT = 16384 + 6144;
constexpr int K1_NS = 5;
constexpr int K1_VOFF = K1_SLOT * K1_NS;
constexpr int K1_LDS = K1_VOFF + 49152;
#define SB0() __builtin_amdgcn_sched_barrier(0)

struct F1 { s16x8 a[8]; s16x8 b[3]; };

__global__ __launch_bounds__(512, 2) void k1_qattn(
    const u16* __restrict__ x0, const u16* __restrict__ kc0, const u16* __restrict__ vc0, const u16* __restrict__ wq0,
    const u16* __restrict__ x1, const u16* __restrict__ kc1, const u16* __restrict__ vc1, const u16* __restrict__ wq1,
    u16* __restrict__ O) {
  extern __shared__ __attribute__((aligned(16))) char smem[];
  const unsigned lds0 = (unsigned)(uintptr_t)smem;
  const int id = blockIdx.x;
  const int xcd = id & 7, slot = id >> 3;
  const int blk = slot >> 4, head = 2 * xcd + ((slot >> 3) & 1), bp = slot & 7, kvh = xcd;
  const int tid = threadIdx.x, lane = tid & 63;
  const int w8 = __builtin_amdgcn_readfirstlane(tid >> 6);
  const int w = w8 & 3, hf = w8 >> 2;
  const int bsel = w >> 1, dh = w & 1;
  const int g = lane >> 4, i16 = lane & 15, q4 = (lane >> 2) & 3, p4 = lane & 3;

  if (hf == 1) {
    const char* Xs = (const char*)((blk ? x1 : x0) + (size_t)bp * 96 * 448);
    const char* Ws = (const char*)((blk ? wq1 : wq0) + head * 256);
    const char* Vs = (const char*)((blk ? vc1 : vc0) + (size_t)((bp * 2) * 8 + kvh) * 48 * 256);
    int wq_off[4];
#pragma unroll
    for (int i = 0; i < 4; ++i) {
      int row = (w * 4 + i) * 2 + (lane >> 5);
      int c16 = lane & 31;
      int f = (row & 3) | ((row >> 1) & 4);
      wq_off[i] = row * 8192 + (((c16 >> 1) ^ f) * 32) + (c16 & 1) * 16;
    }
    int x_off[2];
#pragma unroll
    for (int j = 0; j < 2; ++j) {
      int row = (w * 2 + j) * 12 + (lane < 48 ? (lane >> 2) : 0);
      int lc = (lane & 3) ^ ((-(row >> 2)) & 3);
      x_off[j] = row * 896 + lc * 16;
    }
    int islot = 0;
    auto issue_chunk = [&]() {
      unsigned sb = lds0 + islot * K1_SLOT;
      glds_4p2(Ws, wq_off[0], wq_off[1], wq_off[2], wq_off[3], sb + w * 4096, Xs, x_off[0], x_off[1], sb + 16384 + w * 1536,
               0x0000FFFFFFFFFFFFull);
      Ws += 32 * 8192; Xs += 64;
      islot = (islot == K1_NS - 1) ? 0 : islot + 1;
    };
#pragma unroll 1
    for (int c = 0; c < 5; ++c) issue_chunk();
    WAITV(24);
    BAR();
#pragma unroll 1
    for (int c = 0; c < 14; ++c) {
      if (c >= 12) WAITV(12); else WAITV(18);
      BAR();
      if (c < 9) issue_chunk();
      else if (c < 11) {
        int vo[6];
#pragma unroll
        for (int i = 0; i < 6; ++i) {
          int row = (w * 6 + i) * 2 + (lane >> 5);
          int c16 = lane & 31;
          vo[i] = row * 512 + (((c16 >> 1) ^ (row & 7)) * 32) + (c16 & 1) * 16;
        }
        glds_6(Vs + (size_t)(c - 9) * 8 * 48 * 512, vo[0], vo[1], vo[2], vo[3], vo[4], vo[5],
               lds0 + K1_VOFF + (c - 9) * 24576 + w * 6144);
      }
    }
    WAITV(0);
  } else {
    const u16* Kc = (blk ? kc1 : kc0) + (size_t)((bp * 2 + bsel) * 8 + kvh) * 48 * 256;
    s16x4 kf[3][4][2];
#pragma unroll
    for (int kt = 0; kt < 3; ++kt) {
      const u16* p = Kc + (kt * 16 + i16) * 256 + dh * 128 + 4 * g;
#pragma unroll
      for (int s = 0; s < 4; ++s) {
        kf[kt][s][0] = *(const s16x4*)(p + 32 * s);
        kf[kt][s][1] = *(const s16x4*)(p + 32 * s + 16);
      }
    }
    f32x4 acc[8][3];
#pragma unroll
    for (int a = 0; a < 8; ++a)
#pragma unroll
      for (int b = 0; b < 3; ++b) acc[a][b] = (f32x4){0.f, 0.f, 0.f, 0.f};
    const int fl = q4 | ((g & 1) << 2);
    const int a_base = (8 * g + q4) * 512 + p4 * 8 + dh * 256;
    const int b_base = 16384 + (bsel * 48 + i16) * 64 + ((g ^ ((-(i16 >> 2)) & 3)) * 16);
    int cslot = 0;
    auto rd = [&](F1& f) {
      lds_cptr sb = (lds_cptr)(smem + cslot * K1_SLOT);
      cslot = (cslot == K1_NS - 1) ? 0 : cslot + 1;
#pragma unroll
      for (int tt = 0; tt < 3; ++tt) f.b[tt] = ld_b128(sb + b_base + tt * 16 * 64);
#pragma unroll
      for (int dt = 0; dt < 8; ++dt) {
        lds_cptr ap = sb + a_base + (dt ^ fl) * 32;
        f.a[dt] = cat4(tr_read(ap), tr_read(ap + 2048));
      }
    };
    auto mm = [&](F1& f) {
#pragma unroll
      for (int dt = 0; dt < 8; ++dt)
#pragma unroll
        for (int tt = 0; tt < 3; ++tt) acc[dt][tt] = __builtin_amdgcn_mfma_f32_16x16x32_bf16(f.a[dt], f.b[tt], acc[dt][tt], 0, 0, 0);
    };
    F1 f0, f1;
    BAR();
    rd(f0);
#pragma unroll 1
    for (int c = 0; c < 14; c += 2) {
      BAR(); SB0();
      rd(f1); SB0();
      mm(f0); SB0();
      BAR(); SB0();
      if (c < 12) rd(f0);
      SB0();
      mm(f1); SB0();
    }
    f32x4 sacc[3][3];
#pragma unroll
    for (int a = 0; a < 3; ++a)
#pragma unroll
      for (int b = 0; b < 3; ++b) sacc[a][b] = (f32x4){0.f, 0.f, 0.f, 0.f};
#pragma unroll
    for (int s = 0; s < 4; ++s) {
      s16x8 qf[3];
#pragma unroll
      for (int tt = 0; tt < 3; ++tt) {
#pragma unroll
        for (int r = 0; r < 4; ++r) { qf[tt][r] = f2bf(acc[2 * s][tt][r]); qf[tt][4 + r] = f2bf(acc[2 * s + 1][tt][r]); }
      }
#pragma unroll
      for (int kt = 0; kt < 3; ++kt) {
        s16x8 ka = cat4(kf[kt][s][0], kf[kt][s][1]);
#pragma unroll
        for (int tt = 0; tt < 3; ++tt) sacc[kt][tt] = __builtin_amdgcn_mfma_f32_16x16x32_bf16(ka, qf[tt], sacc[kt][tt], 0, 0, 0);
      }
    }
    f32x4* ex = (f32x4*)(smem + w * 9216) + lane;
#pragma unroll
    for (int kt = 0; kt < 3; ++kt)
#pragma unroll
      for (int tt = 0; tt < 3; ++tt) ex[(tt * 3 + kt) * 64] = sacc[kt][tt];
  }
  BAR();
  const f32x4* exa = (const f32x4*)(smem + (w & 2) * 9216) + lane;
  lds_cptr vb = (lds_cptr)(smem + K1_VOFF + bsel * 24576) + (4 * g + q4) * 512 + p4 * 8;
  const int vsw = (4 * g + q4) & 7;
  s16x4 vf[4][3];
#pragma unroll
  for (int dt = 0; dt < 4; ++dt)
#pragma unroll
    for (int kt = 0; kt < 3; ++kt) vf[dt][kt] = tr_read(vb + (((dh * 8 + hf * 4 + dt) ^ vsw) * 32) + kt * 8192);
  char* ost = smem + 40960 + w * 13056;
#pragma unroll 1
  for (int tt = 0; tt < 3; ++tt) {
    float v[12];
    float m = -INFINITY;
#pragma unroll
    for (int kt = 0; kt < 3; ++kt) {
      f32x4 sv = exa[(tt * 3 + kt) * 64] + exa[(tt * 3 + kt) * 64 + 576];
#pragma unroll
      for (int r = 0; r < 4; ++r) { v[kt * 4 + r] = bf2f(f2bf(sv[r])) * 0.0625f; m = fmaxf(m, v[kt * 4 + r]); }
    }
    m = fmaxf(m, __shfl_xor(m, 16));
    m = fmaxf(m, __shfl_xor(m, 32));
    float sum = 0.f;
#pragma unroll
    for (int e = 0; e < 12; ++e) { v[e] = __expf(v[e] - m); sum += v[e]; }
    sum += __shfl_xor(sum, 16);
    sum += __shfl_xor(sum, 32);
    const float inv = __builtin_amdgcn_rcpf(sum);
    s16x4 pf[3];
#pragma unroll
    for (int kt = 0; kt < 3; ++kt)
#pragma unroll
      for (int r = 0; r < 4; ++r) pf[kt][r] = f2bf(v[kt * 4 + r] * inv);
    char* orow = ost + (tt * 16 + i16) * 272 + hf * 128 + 8 * g;
#pragma unroll
    for (int dt = 0; dt < 4; ++dt) {
      f32x4 oacc = (f32x4){0.f, 0.f, 0.f, 0.f};
#pragma unroll
      for (int kt = 0; kt < 3; ++kt) oacc = __builtin_amdgcn_mfma_f32_16x16x16bf16_1k(vf[dt][kt], pf[kt], oacc, 0, 0, 0);
      s16x4 o;
#pragma unroll
      for (int r = 0; r < 4; ++r) o[r] = f2bf(oacc[r]);
      *(s16x4*)(orow + dt * 32) = o;
    }
  }
  BAR();
  {
    u16* Og = O + (size_t)(blk * 768 + bp * 96 + bsel * 48 + hf * 24 + g) * 4096 + head * 256 + dh * 128 + i16 * 8;
    const char* rp = ost + (hf * 24 + g) * 272 + i16 * 16;
#pragma unroll
    for (int j = 0; j < 6; ++j) *(s16x8*)(Og + (size_t)j * 4 * 4096) = *(const s16x8*)(rp + j * 4 * 272);
  }
}

constexpr int K2_SLOT = 16384 + 12288;
constexpr int K2_NS = 5;
constexpr int K2_LDS = K2_SLOT * K2_NS;
struct F2 { s16x8 a[7]; s16x8 b[3]; };

__global__ __launch_bounds__(512, 2) void k2_oproj(const u16* __restrict__ O, const u16* __restrict__ wo0,
                                                   const u16* __restrict__ wo1, float* __restrict__ part) {
  extern __shared__ __attribute__((aligned(16))) char smem[];
  const unsigned lds0 = (unsigned)(uintptr_t)smem;
  const int id = blockIdx.x;
  const int xcd = id & 7, slot = id >> 3;
  const int blk = xcd >> 2, ks = xcd & 3, mt = slot >> 2, nt = slot & 3;
  const int tid = threadIdx.x, lane = tid & 63;
  const int w8 = __builtin_amdgcn_readfirstlane(tid >> 6);
  const int w = w8 & 3, hf = w8 >> 2;
  const int th = w & 1, kh = w >> 1;
  const int g = lane >> 4, i16 = lane & 15, q4 = (lane >> 2) & 3, p4 = lane & 3;

  if (hf == 1) {
    const char* Ws = (const char*)((blk ? wo1 : wo0) + (size_t)ks * 1024 * 448 + nt * 112);
    const char* Os = (const char*)(O + (size_t)(blk * 768 + mt * 96) * 4096 + ks * 1024);
    int wo_off[4]; u64 wo_act[4];
#pragma unroll
    for (int i = 0; i < 4; ++i) {
      int row = (w * 4 + i) * 4 + (lane >> 4);
      int c16 = lane & 15;
      int f = (row & 3) | ((row >> 1) & 4);
      int ls = (c16 >> 1) ^ f;
      wo_act[i] = __ballot(ls < 7);
      wo_off[i] = row * 896 + (ls < 7 ? ls : 0) * 32 + (c16 & 1) * 16;
    }
    int o_off[3];
#pragma unroll
    for (int j = 0; j < 3; ++j) {
      int row = (w * 3 + j) * 8 + (lane >> 3);
      int lc = (lane & 7) ^ (row & 7);
      o_off[j] = row * 8192 + lc * 16;
    }
    int islot = 0;
    auto issue_chunk = [&]() {
      unsigned sb = lds0 + islot * K2_SLOT;
      glds_4m3(Ws, wo_off[0], wo_off[1], wo_off[2], wo_off[3], sb + w * 4096, wo_act[0], wo_act[1], wo_act[2], wo_act[3], Os,
               o_off[0], o_off[1], o_off[2], sb + 16384 + w * 3072);
      Ws += 64 * 896; Os += 128;
      islot = (islot == K2_NS - 1) ? 0 : islot + 1;
    };
#pragma unroll 1
    for (int c = 0; c < 5; ++c) issue_chunk();
    WAITV(28);
    BAR();
#pragma unroll 1
    for (int c = 0; c < 16; ++c) {
      if (c <= 11) WAITV(21); else if (c == 12) WAITV(14); else if (c == 13) WAITV(7); else WAITV(0);
      BAR();
      if (c < 11) issue_chunk();
    }
    BAR();
    BAR();
  } else {
    f32x4 acc[7][3];
#pragma unroll
    for (int a = 0; a < 7; ++a)
#pragma unroll
      for (int b = 0; b < 3; ++b) acc[a][b] = (f32x4){0.f, 0.f, 0.f, 0.f};
    const int fl = q4 | ((g & 1) << 2);
    const int a_base = (kh * 32 + 8 * g + q4) * 256 + p4 * 8;
    const int brow = th * 48 + i16;
    const int b_base = 16384 + brow * 128 + (((kh * 4 + g) ^ (brow & 7)) * 16);
    int cslot = 0;
    auto rd = [&](F2& f) {
      lds_cptr sb = (lds_cptr)(smem + cslot * K2_SLOT);
      cslot = (cslot == K2_NS - 1) ? 0 : cslot + 1;
#pragma unroll
      for (int tt = 0; tt < 3; ++tt) f.b[tt] = ld_b128(sb + b_base + tt * 16 * 128);
#pragma unroll
      for (int n7 = 0; n7 < 7; ++n7) {
        lds_cptr ap = sb + a_base + ((n7 ^ fl) * 32);
        f.a[n7] = cat4(tr_read(ap), tr_read(ap + 1024));
      }
    };
    auto mm = [&](F2& f) {
#pragma unroll
      for (int n7 = 0; n7 < 7; ++n7)
#pragma unroll
        for (int tt = 0; tt < 3; ++tt) acc[n7][tt] = __builtin_amdgcn_mfma_f32_16x16x32_bf16(f.a[n7], f.b[tt], acc[n7][tt], 0, 0, 0);
    };
    F2 f0, f1;
    BAR();
    rd(f0);
#pragma unroll 1
    for (int c = 0; c < 16; c += 2) {
      BAR(); SB0();
      rd(f1); SB0();
      mm(f0); SB0();
      BAR(); SB0();
      if (c < 14) rd(f0);
      SB0();
      mm(f1); SB0();
    }
    BAR();
    char* ex = smem + 2 * K2_SLOT + th * 21 * 1024;
    if (kh == 1) {
#pragma unroll
      for (int n7 = 0; n7 < 7; ++n7)
#pragma unroll
        for (int tt = 0; tt < 3; ++tt) *((f32x4*)(ex + (n7 * 3 + tt) * 1024) + lane) = acc[n7][tt];
    }
    BAR();
    if (kh == 0) {
      float* dst = part + (size_t)((blk * 4 + ks) * 768 + mt * 96 + th * 48 + i16) * 448 + nt * 112 + 4 * g;
#pragma unroll
      for (int n7 = 0; n7 < 7; ++n7)
#pragma unroll
        for (int tt = 0; tt < 3; ++tt) {
          f32x4 v = acc[n7][tt] + *((const f32x4*)(ex + (n7 * 3 + tt) * 1024) + lane);
          *(f32x4*)(dst + (size_t)tt * 16 * 448 + n7 * 16) = v;
        }
    }
  }
}

__global__ __launch_bounds__(256) void k3_reduce(const float* __restrict__ part, u16* __restrict__ out) {
  int idx = blockIdx.x * 256 + threadIdx.x;
  if (idx >= 2 * 768 * 112) return;
  int n4 = idx % 112, t = (idx / 112) % 768, blk = idx / (112 * 768);
  f32x4 s = (f32x4){0.f, 0.f, 0.f, 0.f};
#pragma unroll
  for (int ks = 0; ks < 4; ++ks) s += *(const f32x4*)(part + (size_t)((blk * 4 + ks) * 768 + t) * 448 + n4 * 4);
  s16x4 o;
#pragma unroll
  for (int r = 0; r < 4; ++r) o[r] = f2bf(s[r]);
  *(s16x4*)(out + (size_t)t * 896 + blk * 448 + n4 * 4) = o;
}

extern "C" void kernel_launch(void* const* d_in, const int* in_sizes, int n_in, void* d_out, int out_size, void* d_ws,
                              size_t ws_size, hipStream_t stream) {
  const u16* x0 = (const u16*)d_in[0];
  const u16* k0 = (const u16*)d_in[1];
  const u16* v0 = (const u16*)d_in[2];
  const u16* wq0 = (const u16*)d_in[3];
  const u16* wo0 = (const u16*)d_in[4];
  const u16* x1 = (const u16*)d_in[5];
  const u16* k1 = (const u16*)d_in[6];
  const u16* v1 = (const u16*)d_in[7];
  const u16* wq1 = (const u16*)d_in[8];
  const u16* wo1 = (const u16*)d_in[9];
  u16* O = (u16*)d_ws;
  float* part = (float*)((char*)d_ws + (size_t)2 * 768 * 4096 * 2);
  (void)hipFuncSetAttribute((const void*)k1_qattn, hipFuncAttributeMaxDynamicSharedMemorySize, K1_LDS);
  (void)hipFuncSetAttribute((const void*)k2_oproj, hipFuncAttributeMaxDynamicSharedMemorySize, K2_LDS);
  k1_qattn<<<256, 512, K1_LDS, stream>>>(x0, k0, v0, wq0, x1, k1, v1, wq1, O);
  k2_oproj<<<256, 512, K2_LDS, stream>>>(O, wo0, wo1, part);
  k3_reduce<<<(2 * 768 * 112 + 255) / 256, 256, 0, stream>>>(part, (u16*)d_out);
}
```

```cpp
#include <hip/hip_runtime.h>
#include <hip/hip_bf16.h>
#include <stdint.h>


typedef __attribute__((ext_vector_type(8))) short s16x8;
typedef __attribute__((ext_vector_type(4))) short s16x4;
typedef __attribute__((ext_vector_type(4))) float f32x4;
typedef __attribute__((address_space(3))) const char* lds_cptr;
typedef unsigned short u16;
typedef unsigned long long u64;

#define WAITV(n) asm volatile("s_waitcnt vmcnt(" #n ")" ::: "memory")
#define BAR() do { asm volatile("s_waitcnt lgkmcnt(0)" ::: "memory"); __builtin_amdgcn_s_barrier(); } while (0)

__device__ __forceinline__ s16x4 tr_read(lds_cptr p) {
  return __builtin_bit_cast(s16x4, __builtin_amdgcn_ds_read_tr16_b64_v4i16((__attribute__((address_space(3))) s16x4*)p));
}
__device__ __forceinline__ s16x8 ld_b128(lds_cptr p) { return *(const __attribute__((address_space(3))) s16x8*)p; }
__device__ __forceinline__ short f2bf(float f) { return __builtin_bit_cast(short, (__bf16)f); }
__device__ __forceinline__ float bf2f(short s) { return __builtin_bit_cast(float, ((unsigned)(u16)s) << 16); }
__device__ __forceinline__ s16x8 cat4(s16x4 a, s16x4 b) { return __builtin_shufflevector(a, b, 0, 1, 2, 3, 4, 5, 6, 7); }

#define GL "global_load_lds_dwordx4 "
__device__ __forceinline__ void glds_4p2(const void* sa, int a0, int a1, int a2, int a3, unsigned la,
                                         const void* sbp, int b0, int b1, unsigned lb, u64 mask) {
  unsigned sv; u64 se;
  asm volatile(
      "s_mov_b32 %0, m0\n\ts_mov_b32 m0, %8\n\ts_nop 0\n\t" GL "%2, %6\n\t"
      "s_add_u32 m0, m0, 0x400\n\ts_nop 0\n\t" GL "%3, %6\n\t"
      "s_add_u32 m0, m0, 0x400\n\ts_nop 0\n\t" GL "%4, %6\n\t"
      "s_add_u32 m0, m0, 0x400\n\ts_nop 0\n\t" GL "%5, %6\n\t"
      "s_mov_b64 %1, exec\n\ts_mov_b64 exec, %12\n\t"
      "s_mov_b32 m0, %11\n\ts_nop 0\n\t" GL "%9, %7\n\t"
      "s_add_u32 m0, m0, 0x300\n\ts_nop 0\n\t" GL "%10, %7\n\t"
      "s_mov_b64 exec, %1\n\ts_mov_b32 m0, %0"
      : "=&s"(sv), "=&s"(se)
      : "v"(a0), "v"(a1), "v"(a2), "v"(a3), "s"(sa), "s"(sbp), "s"(la), "v"(b0), "v"(b1), "s"(lb), "s"(mask)
      : "memory", "scc");
}
__device__ __forceinline__ void glds_4m3(const void* sa, int a0, int a1, int a2, int a3, unsigned la, u64 m0_, u64 m1_,
                                         u64 m2_, u64 m3_, const void* sbp, int b0, int b1, int b2, unsigned lb) {
  unsigned sv; u64 se;
  asm volatile(
      "s_mov_b32 %0, m0\n\ts_mov_b64 %1, exec\n\t"
      "s_mov_b32 m0, %8\n\ts_mov_b64 exec, %13\n\t" GL "%2, %6\n\t"
      "s_add_u32 m0, m0, 0x400\n\ts_mov_b64 exec, %14\n\t" GL "%3, %6\n\t"
      "s_add_u32 m0, m0, 0x400\n\ts_mov_b64 exec, %15\n\t" GL "%4, %6\n\t"
      "s_add_u32 m0, m0, 0x400\n\ts_mov_b64 exec, %16\n\t" GL "%5, %6\n\t"
      "s_mov_b32 m0, %12\n\ts_mov_b64 exec, %1\n\t" GL "%9, %7\n\t"
      "s_add_u32 m0, m0, 0x400\n\ts_nop 0\n\t" GL "%10, %7\n\t"
      "s_add_u32 m0, m0, 0x400\n\ts_nop 0\n\t" GL "%11, %7\n\t"
      "s_mov_b32 m0, %0"
      : "=&s"(sv), "=&s"(se)
      : "v"(a0), "v"(a1), "v"(a2), "v"(a3), "s"(sa), "s"(sbp), "s"(la), "v"(b0), "v"(b1), "v"(b2), "s"(lb), "s"(m0_),
        "s"(m1_), "s"(m2_), "s"(m3_)
      : "memory", "scc");
}
__device__ __forceinline__ void glds_6(const void* sa, int a0, int a1, int a2, int a3, int a4, int a5, unsigned la) {
  unsigned sv;
  asm volatile(
      "s_mov_b32 %0, m0\n\ts_mov_b32 m0, %8\n\ts_nop 0\n\t" GL "%1, %7\n\t"
      "s_add_u32 m0, m0, 0x400\n\ts_nop 0\n\t" GL "%2, %7\n\t"
      "s_add_u32 m0, m0, 0x400\n\ts_nop 0\n\t" GL "%3, %7\n\t"
      "s_add_u32 m0, m0, 0x400\n\ts_nop 0\n\t" GL "%4, %7\n\t"
      "s_add_u32 m0, m0, 0x400\n\ts_nop 0\n\t" GL "%5, %7\n\t"
      "s_add_u32 m0, m0, 0x400\n\ts_nop 0\n\t" GL "%6, %7\n\t"
      "s_mov_b32 m0, %0"
      : "=&s"(sv)
      : "v"(a0), "v"(a1), "v"(a2), "v"(a3), "v"(a4), "v"(a5), "s"(sa), "s"(la)
      : "memory", "scc");
}

constexpr int K1_SLOT = 16384 + 6144;
constexpr int K1_NS = 5;
constexpr int K1_VOFF = K1_SLOT * K1_NS;
constexpr int K1_LDS = K1_VOFF + 49152;
#define SB0() __builtin_amdgcn_sched_barrier(0)

struct F1 { s16x8 a[8]; s16x8 b[3]; };

__global__ __launch_bounds__(512, 2) void k1_qattn(
    const u16* __restrict__ x0, const u16* __restrict__ kc0, const u16* __restrict__ vc0, const u16* __restrict__ wq0,
    const u16* __restrict__ x1, const u16* __restrict__ kc1, const u16* __restrict__ vc1, const u16* __restrict__ wq1,
    u16* __restrict__ O, unsigned* __restrict__ cnt) {
  extern __shared__ __attribute__((aligned(16))) char smem[];
  const unsigned lds0 = (unsigned)(uintptr_t)smem;
  const int id = blockIdx.x;
  if (id == 0 && threadIdx.x < 64) cnt[threadIdx.x] = 0u;
  const int xcd = id & 7, slot = id >> 3;
  const int blk = slot >> 4, head = 2 * xcd + ((slot >> 3) & 1), bp = slot & 7, kvh = xcd;
  const int tid = threadIdx.x, lane = tid & 63;
  const int w8 = __builtin_amdgcn_readfirstlane(tid >> 6);
  const int w = w8 & 3, hf = w8 >> 2;
  const int bsel = w >> 1, dh = w & 1;
  const int g = lane >> 4, i16 = lane & 15, q4 = (lane >> 2) & 3, p4 = lane & 3;

  if (hf == 1) {
    const char* Xs = (const char*)((blk ? x1 : x0) + (size_t)bp * 96 * 448);
    const char* Ws = (const char*)((blk ? wq1 : wq0) + head * 256);
    const char* Vs = (const char*)((blk ? vc1 : vc0) + (size_t)((bp * 2) * 8 + kvh) * 48 * 256);
    int wq_off[4];
#pragma unroll
    for (int i = 0; i < 4; ++i) {
      int row = (w * 4 + i) * 2 + (lane >> 5);
      int c16 = lane & 31;
      int f = (row & 3) | ((row >> 1) & 4);
      wq_off[i] = row * 8192 + (((c16 >> 1) ^ f) * 32) + (c16 & 1) * 16;
    }
    int x_off[2];
#pragma unroll
    for (int j = 0; j < 2; ++j) {
      int row = (w * 2 + j) * 12 + (lane < 48 ? (lane >> 2) : 0);
      int lc = (lane & 3) ^ ((-(row >> 2)) & 3);
      x_off[j] = row * 896 + lc * 16;
    }
    int islot = 0;
    auto issue_chunk = [&]() {
      unsigned sb = lds0 + islot * K1_SLOT;
      glds_4p2(Ws, wq_off[0], wq_off[1], wq_off[2], wq_off[3], sb + w * 4096, Xs, x_off[0], x_off[1], sb + 16384 + w * 1536,
               0x0000FFFFFFFFFFFFull);
      Ws += 32 * 8192; Xs += 64;
      islot = (islot == K1_NS - 1) ? 0 : islot + 1;
    };
#pragma unroll 1
    for (int c = 0; c < 5; ++c) issue_chunk();
    WAITV(24);
    BAR();
#pragma unroll 1
    for (int c = 0; c < 14; ++c) {
      if (c >= 12) WAITV(12); else WAITV(18);
      BAR();
      if (c < 9) issue_chunk();
      else if (c < 11) {
        int vo[6];
#pragma unroll
        for (int i = 0; i < 6; ++i) {
          int row = (w * 6 + i) * 2 + (lane >> 5);
          int c16 = lane & 31;
          vo[i] = row * 512 + (((c16 >> 1) ^ (row & 7)) * 32) + (c16 & 1) * 16;
        }
        glds_6(Vs + (size_t)(c - 9) * 8 * 48 * 512, vo[0], vo[1], vo[2], vo[3], vo[4], vo[5],
               lds0 + K1_VOFF + (c - 9) * 24576 + w * 6144);
      }
    }
    WAITV(0);
  } else {
    const u16* Kc = (blk ? kc1 : kc0) + (size_t)((bp * 2 + bsel) * 8 + kvh) * 48 * 256;
    s16x4 kf[3][4][2];
#pragma unroll
    for (int kt = 0; kt < 3; ++kt) {
      const u16* p = Kc + (kt * 16 + i16) * 256 + dh * 128 + 4 * g;
#pragma unroll
      for (int s = 0; s < 4; ++s) {
        kf[kt][s][0] = *(const s16x4*)(p + 32 * s);
        kf[kt][s][1] = *(const s16x4*)(p + 32 * s + 16);
      }
    }
    f32x4 acc[8][3];
#pragma unroll
    for (int a = 0; a < 8; ++a)
#pragma unroll
      for (int b = 0; b < 3; ++b) acc[a][b] = (f32x4){0.f, 0.f, 0.f, 0.f};
    const int fl = q4 | ((g & 1) << 2);
    const int a_base = (8 * g + q4) * 512 + p4 * 8 + dh * 256;
    const int b_base = 16384 + (bsel * 48 + i16) * 64 + ((g ^ ((-(i16 >> 2)) & 3)) * 16);
    int cslot = 0;
    auto rd = [&](F1& f) {
      lds_cptr sb = (lds_cptr)(smem + cslot * K1_SLOT);
      cslot = (cslot == K1_NS - 1) ? 0 : cslot + 1;
#pragma unroll
      for (int tt = 0; tt < 3; ++tt) f.b[tt] = ld_b128(sb + b_base + tt * 16 * 64);
#pragma unroll
      for (int dt = 0; dt < 8; ++dt) {
        lds_cptr ap = sb + a_base + (dt ^ fl) * 32;
        f.a[dt] = cat4(tr_read(ap), tr_read(ap + 2048));
      }
    };
    auto mm = [&](F1& f) {
#pragma unroll
      for (int dt = 0; dt < 8; ++dt)
#pragma unroll
        for (int tt = 0; tt < 3; ++tt) acc[dt][tt] = __builtin_amdgcn_mfma_f32_16x16x32_bf16(f.a[dt], f.b[tt], acc[dt][tt], 0, 0, 0);
    };
    F1 f0, f1;
    BAR();
    rd(f0);
#pragma unroll 1
    for (int c = 0; c < 14; c += 2) {
      BAR(); SB0();
      rd(f1); SB0();
      mm(f0); SB0();
      BAR(); SB0();
      if (c < 12) rd(f0);
      SB0();
      mm(f1); SB0();
    }
    f32x4 sacc[3][3];
#pragma unroll
    for (int a = 0; a < 3; ++a)
#pragma unroll
      for (int b = 0; b < 3; ++b) sacc[a][b] = (f32x4){0.f, 0.f, 0.f, 0.f};
#pragma unroll
    for (int s = 0; s < 4; ++s) {
      s16x8 qf[3];
#pragma unroll
      for (int tt = 0; tt < 3; ++tt) {
#pragma unroll
        for (int r = 0; r < 4; ++r) { qf[tt][r] = f2bf(acc[2 * s][tt][r]); qf[tt][4 + r] = f2bf(acc[2 * s + 1][tt][r]); }
      }
#pragma unroll
      for (int kt = 0; kt < 3; ++kt) {
        s16x8 ka = cat4(kf[kt][s][0], kf[kt][s][1]);
#pragma unroll
        for (int tt = 0; tt < 3; ++tt) sacc[kt][tt] = __builtin_amdgcn_mfma_f32_16x16x32_bf16(ka, qf[tt], sacc[kt][tt], 0, 0, 0);
      }
    }
    f32x4* ex = (f32x4*)(smem + w * 9216) + lane;
#pragma unroll
    for (int kt = 0; kt < 3; ++kt)
#pragma unroll
      for (int tt = 0; tt < 3; ++tt) ex[(tt * 3 + kt) * 64] = sacc[kt][tt];
  }
  BAR();
  const f32x4* exa = (const f32x4*)(smem + (w & 2) * 9216) + lane;
  lds_cptr vb = (lds_cptr)(smem + K1_VOFF + bsel * 24576) + (4 * g + q4) * 512 + p4 * 8;
  const int vsw = (4 * g + q4) & 7;
  s16x4 vf[4][3];
#pragma unroll
  for (int dt = 0; dt < 4; ++dt)
#pragma unroll
    for (int kt = 0; kt < 3; ++kt) vf[dt][kt] = tr_read(vb + (((dh * 8 + hf * 4 + dt) ^ vsw) * 32) + kt * 8192);
  char* ost = smem + 40960 + w * 13056;
#pragma unroll 1
  for (int tt = 0; tt < 3; ++tt) {
    float v[12];
    float m = -INFINITY;
#pragma unroll
    for (int kt = 0; kt < 3; ++kt) {
      f32x4 sv = exa[(tt * 3 + kt) * 64] + exa[(tt * 3 + kt) * 64 + 576];
#pragma unroll
      for (int r = 0; r < 4; ++r) { v[kt * 4 + r] = bf2f(f2bf(sv[r])) * 0.0625f; m = fmaxf(m, v[kt * 4 + r]); }
    }
    m = fmaxf(m, __shfl_xor(m, 16));
    m = fmaxf(m, __shfl_xor(m, 32));
    float sum = 0.f;
#pragma unroll
    for (int e = 0; e < 12; ++e) { v[e] = __expf(v[e] - m); sum += v[e]; }
    sum += __shfl_xor(sum, 16);
    sum += __shfl_xor(sum, 32);
    const float inv = __builtin_amdgcn_rcpf(sum);
    s16x4 pf[3];
#pragma unroll
    for (int kt = 0; kt < 3; ++kt)
#pragma unroll
      for (int r = 0; r < 4; ++r) pf[kt][r] = f2bf(v[kt * 4 + r] * inv);
    char* orow = ost + (tt * 16 + i16) * 272 + hf * 128 + 8 * g;
#pragma unroll
    for (int dt = 0; dt < 4; ++dt) {
      f32x4 oacc = (f32x4){0.f, 0.f, 0.f, 0.f};
#pragma unroll
      for (int kt = 0; kt < 3; ++kt) oacc = __builtin_amdgcn_mfma_f32_16x16x16bf16_1k(vf[dt][kt], pf[kt], oacc, 0, 0, 0);
      s16x4 o;
#pragma unroll
      for (int r = 0; r < 4; ++r) o[r] = f2bf(oacc[r]);
      *(s16x4*)(orow + dt * 32) = o;
    }
  }
  BAR();
  {
    u16* Og = O + (size_t)(blk * 768 + bp * 96 + bsel * 48 + hf * 24 + g) * 4096 + head * 256 + dh * 128 + i16 * 8;
    const char* rp = ost + (hf * 24 + g) * 272 + i16 * 16;
#pragma unroll
    for (int j = 0; j < 6; ++j) *(s16x8*)(Og + (size_t)j * 4 * 4096) = *(const s16x8*)(rp + j * 4 * 272);
  }
}

constexpr int K2_SLOT = 16384 + 12288;
constexpr int K2_NS = 5;
constexpr int K2_LDS = K2_SLOT * K2_NS;
struct F2 { s16x8 a[7]; s16x8 b[3]; };

__global__ __launch_bounds__(512, 2) void k2_oproj(const u16* __restrict__ O, const u16* __restrict__ wo0,
                                                   const u16* __restrict__ wo1, float* part, unsigned* cnt,
                                                   u16* __restrict__ out) {
  extern __shared__ __attribute__((aligned(16))) char smem[];
  const unsigned lds0 = (unsigned)(uintptr_t)smem;
  const int id = blockIdx.x;
  const int xcd = id & 7, slot = id >> 3;
  const int blk = xcd >> 2, ks = xcd & 3, mt = slot >> 2, nt = slot & 3;
  const int tid = threadIdx.x, lane = tid & 63;
  const int w8 = __builtin_amdgcn_readfirstlane(tid >> 6);
  const int w = w8 & 3, hf = w8 >> 2;
  const int th = w & 1, kh = w >> 1;
  const int g = lane >> 4, i16 = lane & 15, q4 = (lane >> 2) & 3, p4 = lane & 3;

  if (hf == 1) {
    const char* Ws = (const char*)((blk ? wo1 : wo0) + (size_t)ks * 1024 * 448 + nt * 112);
    const char* Os = (const char*)(O + (size_t)(blk * 768 + mt * 96) * 4096 + ks * 1024);
    int wo_off[4]; u64 wo_act[4];
#pragma unroll
    for (int i = 0; i < 4; ++i) {
      int row = (w * 4 + i) * 4 + (lane >> 4);
      int c16 = lane & 15;
      int f = (row & 3) | ((row >> 1) & 4);
      int ls = (c16 >> 1) ^ f;
      wo_act[i] = __ballot(ls < 7);
      wo_off[i] = row * 896 + (ls < 7 ? ls : 0) * 32 + (c16 & 1) * 16;
    }
    int o_off[3];
#pragma unroll
    for (int j = 0; j < 3; ++j) {
      int row = (w * 3 + j) * 8 + (lane >> 3);
      int lc = (lane & 7) ^ (row & 7);
      o_off[j] = row * 8192 + lc * 16;
    }
    int islot = 0;
    auto issue_chunk = [&]() {
      unsigned sb = lds0 + islot * K2_SLOT;
      glds_4m3(Ws, wo_off[0], wo_off[1], wo_off[2], wo_off[3], sb + w * 4096, wo_act[0], wo_act[1], wo_act[2], wo_act[3], Os,
               o_off[0], o_off[1], o_off[2], sb + 16384 + w * 3072);
      Ws += 64 * 896; Os += 128;
      islot = (islot == K2_NS - 1) ? 0 : islot + 1;
    };
#pragma unroll 1
    for (int c = 0; c < 5; ++c) issue_chunk();
    WAITV(28);
    BAR();
#pragma unroll 1
    for (int c = 0; c < 16; ++c) {
      if (c <= 11) WAITV(21); else if (c == 12) WAITV(14); else if (c == 13) WAITV(7); else WAITV(0);
      BAR();
      if (c < 11) issue_chunk();
    }
    BAR();
    BAR();
  } else {
    f32x4 acc[7][3];
#pragma unroll
    for (int a = 0; a < 7; ++a)
#pragma unroll
      for (int b = 0; b < 3; ++b) acc[a][b] = (f32x4){0.f, 0.f, 0.f, 0.f};
    const int fl = q4 | ((g & 1) << 2);
    const int a_base = (kh * 32 + 8 * g + q4) * 256 + p4 * 8;
    const int brow = th * 48 + i16;
    const int b_base = 16384 + brow * 128 + (((kh * 4 + g) ^ (brow & 7)) * 16);
    int cslot = 0;
    auto rd = [&](F2& f) {
      lds_cptr sb = (lds_cptr)(smem + cslot * K2_SLOT);
      cslot = (cslot == K2_NS - 1) ? 0 : cslot + 1;
#pragma unroll
      for (int tt = 0; tt < 3; ++tt) f.b[tt] = ld_b128(sb + b_base + tt * 16 * 128);
#pragma unroll
      for (int n7 = 0; n7 < 7; ++n7) {
        lds_cptr ap = sb + a_base + ((n7 ^ fl) * 32);
        f.a[n7] = cat4(tr_read(ap), tr_read(ap + 1024));
      }
    };
    auto mm = [&](F2& f) {
#pragma unroll
      for (int n7 = 0; n7 < 7; ++n7)
#pragma unroll
        for (int tt = 0; tt < 3; ++tt) acc[n7][tt] = __builtin_amdgcn_mfma_f32_16x16x32_bf16(f.a[n7], f.b[tt], acc[n7][tt], 0, 0, 0);
    };
    F2 f0, f1;
    BAR();
    rd(f0);
#pragma unroll 1
    for (int c = 0; c < 16; c += 2) {
      BAR(); SB0();
      rd(f1); SB0();
      mm(f0); SB0();
      BAR(); SB0();
      if (c < 14) rd(f0);
      SB0();
      mm(f1); SB0();
    }
    BAR();
    char* ex = smem + 2 * K2_SLOT + th * 21 * 1024;
    if (kh == 1) {
#pragma unroll
      for (int n7 = 0; n7 < 7; ++n7)
#pragma unroll
        for (int tt = 0; tt < 3; ++tt) *((f32x4*)(ex + (n7 * 3 + tt) * 1024) + lane) = acc[n7][tt];
    }
    BAR();
    if (kh == 0) {
      float* dst = part + (size_t)((blk * 4 + ks) * 768 + mt * 96 + th * 48 + i16) * 448 + nt * 112 + 4 * g;
#pragma unroll
      for (int n7 = 0; n7 < 7; ++n7)
#pragma unroll
        for (int tt = 0; tt < 3; ++tt) {
          f32x4 v = acc[n7][tt] + *((const f32x4*)(ex + (n7 * 3 + tt) * 1024) + lane);
          asm volatile("global_store_dwordx4 %0, %1, off sc1\n\ts_nop 1" :: "v"(dst + (size_t)tt * 16 * 448 + n7 * 16), "v"(v) : "memory");
        }
      WAITV(0);
    }
  }
  BAR();
  unsigned* tk = (unsigned*)smem;
  if (tid == 0) {
    unsigned t = __hip_atomic_fetch_add(cnt + blk * 32 + mt * 4 + nt, 1u, __ATOMIC_RELAXED, __HIP_MEMORY_SCOPE_AGENT);
    *tk = t;
  }
  BAR();
  if (*(volatile unsigned*)tk != 3u) return;
  {
    f32x4 v[6][4];
    const float* src = part + (size_t)(blk * 4 * 768 + mt * 96) * 448 + nt * 112;
#pragma unroll
    for (int j = 0; j < 6; ++j) {
      int e = tid + 512 * j; if (e > 2687) e = 2687;
      const float* q = src + (e / 28) * 448 + (e % 28) * 4;
#pragma unroll
      for (int k4 = 0; k4 < 4; ++k4)
        asm volatile("global_load_dwordx4 %0, %1, off sc1" : "=&v"(v[j][k4]) : "v"(q + (size_t)k4 * 768 * 448) : "memory");
    }
    asm volatile("s_waitcnt vmcnt(0)" : "+v"(v[0][0]), "+v"(v[0][1]), "+v"(v[0][2]), "+v"(v[0][3]), "+v"(v[1][0]), "+v"(v[1][1]),
                 "+v"(v[1][2]), "+v"(v[1][3]), "+v"(v[2][0]), "+v"(v[2][1]), "+v"(v[2][2]), "+v"(v[2][3]) :: "memory");
    asm volatile("" : "+v"(v[3][0]), "+v"(v[3][1]), "+v"(v[3][2]), "+v"(v[3][3]), "+v"(v[4][0]), "+v"(v[4][1]), "+v"(v[4][2]),
                 "+v"(v[4][3]), "+v"(v[5][0]), "+v"(v[5][1]), "+v"(v[5][2]), "+v"(v[5][3]) :: "memory");
#pragma unroll
    for (int j = 0; j < 6; ++j) {
      int e = tid + 512 * j;
      f32x4 sm = (v[j][0] + v[j][1]) + (v[j][2] + v[j][3]);
      s16x4 o;
#pragma unroll
      for (int r = 0; r < 4; ++r) o[r] = f2bf(sm[r]);
      if (e < 2688) *(s16x4*)(out + (size_t)(mt * 96 + e / 28) * 896 + blk * 448 + nt * 112 + (e % 28) * 4) = o;
    }
  }
}

extern "C" void kernel_launch(void* const* d_in, const int* in_sizes, int n_in, void* d_out, int out_size, void* d_ws,
                              size_t ws_size, hipStream_t stream) {
  const u16* x0 = (const u16*)d_in[0];
  const u16* k0 = (const u16*)d_in[1];
  const u16* v0 = (const u16*)d_in[2];
  const u16* wq0 = (const u16*)d_in[3];
  const u16* wo0 = (const u16*)d_in[4];
  const u16* x1 = (const u16*)d_in[5];
  const u16* k1 = (const u16*)d_in[6];
  const u16* v1 = (const u16*)d_in[7];
  const u16* wq1 = (const u16*)d_in[8];
  const u16* wo1 = (const u16*)d_in[9];
  u16* O = (u16*)d_ws;
  float* part = (float*)((char*)d_ws + (size_t)2 * 768 * 4096 * 2);
  unsigned* cnt = (unsigned*)((char*)d_ws + (size_t)32 * 1024 * 1024);
  (void)hipFuncSetAttribute((const void*)k1_qattn, hipFuncAttributeMaxDynamicSharedMemorySize, K1_LDS);
  (void)hipFuncSetAttribute((const void*)k2_oproj, hipFuncAttributeMaxDynamicSharedMemorySize, K2_LDS);
  k1_qattn<<<256, 512, K1_LDS, stream>>>(x0, k0, v0, wq0, x1, k1, v1, wq1, O, cnt);
  k2_oproj<<<256, 512, K2_LDS, stream>>>(O, wo0, wo1, part, cnt, (u16*)d_out);
}
```

```cpp
#include <hip/hip_runtime.h>
#include <hip/hip_bf16.h>
#include <stdint.h>


typedef __attribute__((ext_vector_type(8))) short s16x8;
typedef __attribute__((ext_vector_type(4))) short s16x4;
typedef __attribute__((ext_vector_type(4))) float f32x4;
typedef __attribute__((address_space(3))) const char* lds_cptr;
typedef unsigned short u16;
typedef unsigned long long u64;

#define WAITV(n) asm volatile("s_waitcnt vmcnt(" #n ")" ::: "memory")
#define BAR() do { asm volatile("s_waitcnt lgkmcnt(0)" ::: "memory"); __builtin_amdgcn_s_barrier(); } while (0)

__device__ __forceinline__ s16x4 tr_read(lds_cptr p) {
  return __builtin_bit_cast(s16x4, __builtin_amdgcn_ds_read_tr16_b64_v4i16((__attribute__((address_space(3))) s16x4*)p));
}
__device__ __forceinline__ s16x8 ld_b128(lds_cptr p) { return *(const __attribute__((address_space(3))) s16x8*)p; }
__device__ __forceinline__ short f2bf(float f) { return __builtin_bit_cast(short, (__bf16)f); }
__device__ __forceinline__ float bf2f(short s) { return __builtin_bit_cast(float, ((unsigned)(u16)s) << 16); }
__device__ __forceinline__ s16x8 cat4(s16x4 a, s16x4 b) { return __builtin_shufflevector(a, b, 0, 1, 2, 3, 4, 5, 6, 7); }

#define GL "global_load_lds_dwordx4 "
__device__ __forceinline__ void glds_4p2(const void* sa, int a0, int a1, int a2, int a3, unsigned la,
                                         const void* sbp, int b0, int b1, unsigned lb, u64 mask) {
  unsigned sv; u64 se;
  asm volatile(
      "s_mov_b32 %0, m0\n\ts_mov_b32 m0, %8\n\ts_nop 0\n\t" GL "%2, %6\n\t"
      "s_add_u32 m0, m0, 0x400\n\ts_nop 0\n\t" GL "%3, %6\n\t"
      "s_add_u32 m0, m0, 0x400\n\ts_nop 0\n\t" GL "%4, %6\n\t"
      "s_add_u32 m0, m0, 0x400\n\ts_nop 0\n\t" GL "%5, %6\n\t"
      "s_mov_b64 %1, exec\n\ts_mov_b64 exec, %12\n\t"
      "s_mov_b32 m0, %11\n\ts_nop 0\n\t" GL "%9, %7\n\t"
      "s_add_u32 m0, m0, 0x300\n\ts_nop 0\n\t" GL "%10, %7\n\t"
      "s_mov_b64 exec, %1\n\ts_mov_b32 m0, %0"
      : "=&s"(sv), "=&s"(se)
      : "v"(a0), "v"(a1), "v"(a2), "v"(a3), "s"(sa), "s"(sbp), "s"(la), "v"(b0), "v"(b1), "s"(lb), "s"(mask)
      : "memory", "scc");
}
__device__ __forceinline__ void glds_4m3(const void* sa, int a0, int a1, int a2, int a3, unsigned la, u64 m0_, u64 m1_,
                                         u64 m2_, u64 m3_, const void* sbp, int b0, int b1, int b2, unsigned lb) {
  unsigned sv; u64 se;
  asm volatile(
      "s_mov_b32 %0, m0\n\ts_mov_b64 %1, exec\n\t"
      "s_mov_b32 m0, %8\n\ts_mov_b64 exec, %13\n\t" GL "%2, %6\n\t"
      "s_add_u32 m0, m0, 0x400\n\ts_mov_b64 exec, %14\n\t" GL "%3, %6\n\t"
      "s_add_u32 m0, m0, 0x400\n\ts_mov_b64 exec, %15\n\t" GL "%4, %6\n\t"
      "s_add_u32 m0, m0, 0x400\n\ts_mov_b64 exec, %16\n\t" GL "%5, %6\n\t"
      "s_mov_b32 m0, %12\n\ts_mov_b64 exec, %1\n\t" GL "%9, %7\n\t"
      "s_add_u32 m0, m0, 0x400\n\ts_nop 0\n\t" GL "%10, %7\n\t"
      "s_add_u32 m0, m0, 0x400\n\ts_nop 0\n\t" GL "%11, %7\n\t"
      "s_mov_b32 m0, %0"
      : "=&s"(sv), "=&s"(se)
      : "v"(a0), "v"(a1), "v"(a2), "v"(a3), "s"(sa), "s"(sbp), "s"(la), "v"(b0), "v"(b1), "v"(b2), "s"(lb), "s"(m0_),
        "s"(m1_), "s"(m2_), "s"(m3_)
      : "memory", "scc");
}
__device__ __forceinline__ void glds_6(const void* sa, int a0, int a1, int a2, int a3, int a4, int a5, unsigned la) {
  unsigned sv;
  asm volatile(
      "s_mov_b32 %0, m0\n\ts_mov_b32 m0, %8\n\ts_nop 0\n\t" GL "%1, %7\n\t"
      "s_add_u32 m0, m0, 0x400\n\ts_nop 0\n\t" GL "%2, %7\n\t"
      "s_add_u32 m0, m0, 0x400\n\ts_nop 0\n\t" GL "%3, %7\n\t"
      "s_add_u32 m0, m0, 0x400\n\ts_nop 0\n\t" GL "%4, %7\n\t"
      "s_add_u32 m0, m0, 0x400\n\ts_nop 0\n\t" GL "%5, %7\n\t"
      "s_add_u32 m0, m0, 0x400\n\ts_nop 0\n\t" GL "%6, %7\n\t"
      "s_mov_b32 m0, %0"
      : "=&s"(sv)
      : "v"(a0), "v"(a1), "v"(a2), "v"(a3), "v"(a4), "v"(a5), "s"(sa), "s"(la)
      : "memory", "scc");
}

constexpr int K1_SLOT = 16384 + 6144;
constexpr int K1_NS = 5;
constexpr int K1_VOFF = K1_SLOT * K1_NS;
constexpr int K1_LDS = K1_VOFF + 49152;
#define SB0() __builtin_amdgcn_sched_barrier(0)

struct F1 { s16x8 a[8]; s16x8 b[3]; };

__global__ __launch_bounds__(512, 2) void k1_qattn(
    const u16* __restrict__ x0, const u16* __restrict__ kc0, const u16* __restrict__ vc0, const u16* __restrict__ wq0,
    const u16* __restrict__ x1, const u16* __restrict__ kc1, const u16* __restrict__ vc1, const u16* __restrict__ wq1,
    u16* __restrict__ O, unsigned* __restrict__ cnt, const u16* __restrict__ wo0, const u16* __restrict__ wo1) {
  extern __shared__ __attribute__((aligned(16))) char smem[];
  const unsigned lds0 = (unsigned)(uintptr_t)smem;
  const int id = blockIdx.x;
  if (id == 0 && threadIdx.x < 64) cnt[threadIdx.x] = 0u;
  const int xcd = id & 7, slot = id >> 3;
  const int blk = slot >> 4, head = 2 * xcd + ((slot >> 3) & 1), bp = slot & 7, kvh = xcd;
  const int tid = threadIdx.x, lane = tid & 63;
  const int w8 = __builtin_amdgcn_readfirstlane(tid >> 6);
  const int w = w8 & 3, hf = w8 >> 2;
  const int bsel = w >> 1, dh = w & 1;
  const int g = lane >> 4, i16 = lane & 15, q4 = (lane >> 2) & 3, p4 = lane & 3;

  if (hf == 1) {
    const char* Xs = (const char*)((blk ? x1 : x0) + (size_t)bp * 96 * 448);
    const char* Ws = (const char*)((blk ? wq1 : wq0) + head * 256);
    const char* Vs = (const char*)((blk ? vc1 : vc0) + (size_t)((bp * 2) * 8 + kvh) * 48 * 256);
    int wq_off[4];
#pragma unroll
    for (int i = 0; i < 4; ++i) {
      int row = (w * 4 + i) * 2 + (lane >> 5);
      int c16 = lane & 31;
      int f = (row & 3) | ((row >> 1) & 4);
      wq_off[i] = row * 8192 + (((c16 >> 1) ^ f) * 32) + (c16 & 1) * 16;
    }
    int x_off[2];
#pragma unroll
    for (int j = 0; j < 2; ++j) {
      int row = (w * 2 + j) * 12 + (lane < 48 ? (lane >> 2) : 0);
      int lc = (lane & 3) ^ ((-(row >> 2)) & 3);
      x_off[j] = row * 896 + lc * 16;
    }
    int islot = 0;
    auto issue_chunk = [&]() {
      unsigned sb = lds0 + islot * K1_SLOT;
      glds_4p2(Ws, wq_off[0], wq_off[1], wq_off[2], wq_off[3], sb + w * 4096, Xs, x_off[0], x_off[1], sb + 16384 + w * 1536,
               0x0000FFFFFFFFFFFFull);
      Ws += 32 * 8192; Xs += 64;
      islot = (islot == K1_NS - 1) ? 0 : islot + 1;
    };
#pragma unroll 1
    for (int c = 0; c < 5; ++c) issue_chunk();
    WAITV(24);
    BAR();
#pragma unroll 1
    for (int c = 0; c < 14; ++c) {
      if (c >= 12) WAITV(19); else WAITV(18);
      BAR();
      if (c < 9) issue_chunk();
      else if (c < 11) {
        int vo[6];
#pragma unroll
        for (int i = 0; i < 6; ++i) {
          int row = (w * 6 + i) * 2 + (lane >> 5);
          int c16 = lane & 31;
          vo[i] = row * 512 + (((c16 >> 1) ^ (row & 7)) * 32) + (c16 & 1) * 16;
        }
        glds_6(Vs + (size_t)(c - 9) * 8 * 48 * 512, vo[0], vo[1], vo[2], vo[3], vo[4], vo[5],
               lds0 + K1_VOFF + (c - 9) * 24576 + w * 6144);
      } else if (c == 11) {
        const char* wp = (const char*)((id >> 7) ? wo1 : wo0) + (size_t)(id & 127) * 28672 + w * 7168 + lane * 16;
#pragma unroll
        for (int i = 0; i < 7; ++i) {
          f32x4 t;
          asm volatile("global_load_dwordx4 %0, %1, off" : "=&v"(t) : "v"(wp + i * 1024) : "memory");
        }
      }
    }
    WAITV(0);
  } else {
    const u16* Kc = (blk ? kc1 : kc0) + (size_t)((bp * 2 + bsel) * 8 + kvh) * 48 * 256;
    s16x4 kf[3][4][2];
#pragma unroll
    for (int kt = 0; kt < 3; ++kt) {
      const u16* p = Kc + (kt * 16 + i16) * 256 + dh * 128 + 4 * g;
#pragma unroll
      for (int s = 0; s < 4; ++s) {
        kf[kt][s][0] = *(const s16x4*)(p + 32 * s);
        kf[kt][s][1] = *(const s16x4*)(p + 32 * s + 16);
      }
    }
    f32x4 acc[8][3];
#pragma unroll
    for (int a = 0; a < 8; ++a)
#pragma unroll
      for (int b = 0; b < 3; ++b) acc[a][b] = (f32x4){0.f, 0.f, 0.f, 0.f};
    const int fl = q4 | ((g & 1) << 2);
    const int a_base = (8 * g + q4) * 512 + p4 * 8 + dh * 256;
    const int b_base = 16384 + (bsel * 48 + i16) * 64 + ((g ^ ((-(i16 >> 2)) & 3)) * 16);
    int cslot = 0;
    auto rd = [&](F1& f) {
      lds_cptr sb = (lds_cptr)(smem + cslot * K1_SLOT);
      cslot = (cslot == K1_NS - 1) ? 0 : cslot + 1;
#pragma unroll
      for (int tt = 0; tt < 3; ++tt) f.b[tt] = ld_b128(sb + b_base + tt * 16 * 64);
#pragma unroll
      for (int dt = 0; dt < 8; ++dt) {
        lds_cptr ap = sb + a_base + (dt ^ fl) * 32;
        f.a[dt] = cat4(tr_read(ap), tr_read(ap + 2048));
      }
    };
    auto mm = [&](F1& f) {
#pragma unroll
      for (int dt = 0; dt < 8; ++dt)
#pragma unroll
        for (int tt = 0; tt < 3; ++tt) acc[dt][tt] = __builtin_amdgcn_mfma_f32_16x16x32_bf16(f.a[dt], f.b[tt], acc[dt][tt], 0, 0, 0);
    };
    F1 f0, f1;
    BAR();
    rd(f0);
#pragma unroll 1
    for (int c = 0; c < 14; c += 2) {
      BAR(); SB0();
      rd(f1); SB0();
      mm(f0); SB0();
      BAR(); SB0();
      if (c < 12) rd(f0);
      SB0();
      mm(f1); SB0();
    }
    f32x4 sacc[3][3];
#pragma unroll
    for (int a = 0; a < 3; ++a)
#pragma unroll
      for (int b = 0; b < 3; ++b) sacc[a][b] = (f32x4){0.f, 0.f, 0.f, 0.f};
#pragma unroll
    for (int s = 0; s < 4; ++s) {
      s16x8 qf[3];
#pragma unroll
      for (int tt = 0; tt < 3; ++tt) {
#pragma unroll
        for (int r = 0; r < 4; ++r) { qf[tt][r] = f2bf(acc[2 * s][tt][r]); qf[tt][4 + r] = f2bf(acc[2 * s + 1][tt][r]); }
      }
#pragma unroll
      for (int kt = 0; kt < 3; ++kt) {
        s16x8 ka = cat4(kf[kt][s][0], kf[kt][s][1]);
#pragma unroll
        for (int tt = 0; tt < 3; ++tt) sacc[kt][tt] = __builtin_amdgcn_mfma_f32_16x16x32_bf16(ka, qf[tt], sacc[kt][tt], 0, 0, 0);
      }
    }
    f32x4* ex = (f32x4*)(smem + w * 9216) + lane;
#pragma unroll
    for (int kt = 0; kt < 3; ++kt)
#pragma unroll
      for (int tt = 0; tt < 3; ++tt) ex[(tt * 3 + kt) * 64] = sacc[kt][tt];
  }
  BAR();
  const f32x4* exa = (const f32x4*)(smem + (w & 2) * 9216) + lane;
  lds_cptr vb = (lds_cptr)(smem + K1_VOFF + bsel * 24576) + (4 * g + q4) * 512 + p4 * 8;
  const int vsw = (4 * g + q4) & 7;
  s16x4 vf[4][3];
#pragma unroll
  for (int dt = 0; dt < 4; ++dt)
#pragma unroll
    for (int kt = 0; kt < 3; ++kt) vf[dt][kt] = tr_read(vb + (((dh * 8 + hf * 4 + dt) ^ vsw) * 32) + kt * 8192);
  char* ost = smem + 40960 + w * 13056;
#pragma unroll 1
  for (int tt = 0; tt < 3; ++tt) {
    float v[12];
    float m = -INFINITY;
#pragma unroll
    for (int kt = 0; kt < 3; ++kt) {
      f32x4 sv = exa[(tt * 3 + kt) * 64] + exa[(tt * 3 + kt) * 64 + 576];
#pragma unroll
      for (int r = 0; r < 4; ++r) { v[kt * 4 + r] = bf2f(f2bf(sv[r])) * 0.0625f; m = fmaxf(m, v[kt * 4 + r]); }
    }
    m = fmaxf(m, __shfl_xor(m, 16));
    m = fmaxf(m, __shfl_xor(m, 32));
    float sum = 0.f;
#pragma unroll
    for (int e = 0; e < 12; ++e) { v[e] = __expf(v[e] - m); sum += v[e]; }
    sum += __shfl_xor(sum, 16);
    sum += __shfl_xor(sum, 32);
    const float inv = __builtin_amdgcn_rcpf(sum);
    s16x4 pf[3];
#pragma unroll
    for (int kt = 0; kt < 3; ++kt)
#pragma unroll
      for (int r = 0; r < 4; ++r) pf[kt][r] = f2bf(v[kt * 4 + r] * inv);
    char* orow = ost + (tt * 16 + i16) * 272 + hf * 128 + 8 * g;
#pragma unroll
    for (int dt = 0; dt < 4; ++dt) {
      f32x4 oacc = (f32x4){0.f, 0.f, 0.f, 0.f};
#pragma unroll
      for (int kt = 0; kt < 3; ++kt) oacc = __builtin_amdgcn_mfma_f32_16x16x16bf16_1k(vf[dt][kt], pf[kt], oacc, 0, 0, 0);
      s16x4 o;
#pragma unroll
      for (int r = 0; r < 4; ++r) o[r] = f2bf(oacc[r]);
      *(s16x4*)(orow + dt * 32) = o;
    }
  }
  BAR();
  {
    u16* Og = O + (size_t)(blk * 768 + bp * 96 + bsel * 48 + hf * 24 + g) * 4096 + head * 256 + dh * 128 + i16 * 8;
    const char* rp = ost + (hf * 24 + g) * 272 + i16 * 16;
#pragma unroll
    for (int j = 0; j < 6; ++j) *(s16x8*)(Og + (size_t)j * 4 * 4096) = *(const s16x8*)(rp + j * 4 * 272);
  }
}

constexpr int K2_SLOT = 16384 + 12288;
constexpr int K2_NS = 5;
constexpr int K2_LDS = K2_SLOT * K2_NS;
struct F2 { s16x8 a[7]; s16x8 b[3]; };

__global__ __launch_bounds__(512, 2) void k2_oproj(const u16* __restrict__ O, const u16* __restrict__ wo0,
                                                   const u16* __restrict__ wo1, float* part, unsigned* cnt,
                                                   u16* __restrict__ out) {
  extern __shared__ __attribute__((aligned(16))) char smem[];
  const unsigned lds0 = (unsigned)(uintptr_t)smem;
  const int id = blockIdx.x;
  const int xcd = id & 7, slot = id >> 3;
  const int blk = xcd >> 2, ks = xcd & 3, mt = slot >> 2, nt = slot & 3;
  const int tid = threadIdx.x, lane = tid & 63;
  const int w8 = __builtin_amdgcn_readfirstlane(tid >> 6);
  const int w = w8 & 3, hf = w8 >> 2;
  const int th = w & 1, kh = w >> 1;
  const int g = lane >> 4, i16 = lane & 15, q4 = (lane >> 2) & 3, p4 = lane & 3;

  if (hf == 1) {
    const char* Ws = (const char*)((blk ? wo1 : wo0) + (size_t)ks * 1024 * 448 + nt * 112);
    const char* Os = (const char*)(O + (size_t)(blk * 768 + mt * 96) * 4096 + ks * 1024);
    int wo_off[4]; u64 wo_act[4];
#pragma unroll
    for (int i = 0; i < 4; ++i) {
      int row = (w * 4 + i) * 4 + (lane >> 4);
      int c16 = lane & 15;
      int f = (row & 3) | ((row >> 1) & 4);
      int ls = (c16 >> 1) ^ f;
      wo_act[i] = __ballot(ls < 7);
      wo_off[i] = row * 896 + (ls < 7 ? ls : 0) * 32 + (c16 & 1) * 16;
    }
    int o_off[3];
#pragma unroll
    for (int j = 0; j < 3; ++j) {
      int row = (w * 3 + j) * 8 + (lane >> 3);
      int lc = (lane & 7) ^ (row & 7);
      o_off[j] = row * 8192 + lc * 16;
    }
    int islot = 0;
    auto issue_chunk = [&]() {
      unsigned sb = lds0 + islot * K2_SLOT;
      glds_4m3(Ws, wo_off[0], wo_off[1], wo_off[2], wo_off[3], sb + w * 4096, wo_act[0], wo_act[1], wo_act[2], wo_act[3], Os,
               o_off[0], o_off[1], o_off[2], sb + 16384 + w * 3072);
      Ws += 64 * 896; Os += 128;
      islot = (islot == K2_NS - 1) ? 0 : islot + 1;
    };
#pragma unroll 1
    for (int c = 0; c < 5; ++c) issue_chunk();
    WAITV(28);
    BAR();
#pragma unroll 1
    for (int c = 0; c < 16; ++c) {
      if (c <= 11) WAITV(21); else if (c == 12) WAITV(14); else if (c == 13) WAITV(7); else WAITV(0);
      BAR();
      if (c < 11) issue_chunk();
    }
    BAR();
    BAR();
  } else {
    f32x4 acc[7][3];
#pragma unroll
    for (int a = 0; a < 7; ++a)
#pragma unroll
      for (int b = 0; b < 3; ++b) acc[a][b] = (f32x4){0.f, 0.f, 0.f, 0.f};
    const int fl = q4 | ((g & 1) << 2);
    const int a_base = (kh * 32 + 8 * g + q4) * 256 + p4 * 8;
    const int brow = th * 48 + i16;
    const int b_base = 16384 + brow * 128 + (((kh * 4 + g) ^ (brow & 7)) * 16);
    int cslot = 0;
    auto rd = [&](F2& f) {
      lds_cptr sb = (lds_cptr)(smem + cslot * K2_SLOT);
      cslot = (cslot == K2_NS - 1) ? 0 : cslot + 1;
#pragma unroll
      for (int tt = 0; tt < 3; ++tt) f.b[tt] = ld_b128(sb + b_base + tt * 16 * 128);
#pragma unroll
      for (int n7 = 0; n7 < 7; ++n7) {
        lds_cptr ap = sb + a_base + ((n7 ^ fl) * 32);
        f.a[n7] = cat4(tr_read(ap), tr_read(ap + 1024));
      }
    };
    auto mm = [&](F2& f) {
#pragma unroll
      for (int n7 = 0; n7 < 7; ++n7)
#pragma unroll
        for (int tt = 0; tt < 3; ++tt) acc[n7][tt] = __builtin_amdgcn_mfma_f32_16x16x32_bf16(f.a[n7], f.b[tt], acc[n7][tt], 0, 0, 0);
    };
    F2 f0, f1;
    BAR();
    rd(f0);
#pragma unroll 1
    for (int c = 0; c < 16; c += 2) {
      BAR(); SB0();
      rd(f1); SB0();
      mm(f0); SB0();
      BAR(); SB0();
      if (c < 14) rd(f0);
      SB0();
      mm(f1); SB0();
    }
    BAR();
    char* ex = smem + 2 * K2_SLOT + th * 21 * 1024;
    if (kh == 1) {
#pragma unroll
      for (int n7 = 0; n7 < 7; ++n7)
#pragma unroll
        for (int tt = 0; tt < 3; ++tt) *((f32x4*)(ex + (n7 * 3 + tt) * 1024) + lane) = acc[n7][tt];
    }
    BAR();
    if (kh == 0) {
      float* dst = part + (size_t)((blk * 4 + ks) * 768 + mt * 96 + th * 48 + i16) * 448 + nt * 112 + 4 * g;
#pragma unroll
      for (int n7 = 0; n7 < 7; ++n7)
#pragma unroll
        for (int tt = 0; tt < 3; ++tt) {
          f32x4 v = acc[n7][tt] + *((const f32x4*)(ex + (n7 * 3 + tt) * 1024) + lane);
          asm volatile("global_store_dwordx4 %0, %1, off sc1\n\ts_nop 1" :: "v"(dst + (size_t)tt * 16 * 448 + n7 * 16), "v"(v) : "memory");
        }
      WAITV(0);
    }
  }
  BAR();
  unsigned* tk = (unsigned*)smem;
  if (tid == 0) {
    unsigned t = __hip_atomic_fetch_add(cnt + blk * 32 + mt * 4 + nt, 1u, __ATOMIC_RELAXED, __HIP_MEMORY_SCOPE_AGENT);
    *tk = t;
  }
  BAR();
  if (*(volatile unsigned*)tk != 3u) return;
  {
    f32x4 v[6][4];
    const float* src = part + (size_t)(blk * 4 * 768 + mt * 96) * 448 + nt * 112;
#pragma unroll
    for (int j = 0; j < 6; ++j) {
      int e = tid + 512 * j; if (e > 2687) e = 2687;
      const float* q = src + (e / 28) * 448 + (e % 28) * 4;
#pragma unroll
      for (int k4 = 0; k4 < 4; ++k4)
        asm volatile("global_load_dwordx4 %0, %1, off sc1" : "=&v"(v[j][k4]) : "v"(q + (size_t)k4 * 768 * 448) : "memory");
    }
    asm volatile("s_waitcnt vmcnt(0)" : "+v"(v[0][0]), "+v"(v[0][1]), "+v"(v[0][2]), "+v"(v[0][3]), "+v"(v[1][0]), "+v"(v[1][1]),
                 "+v"(v[1][2]), "+v"(v[1][3]), "+v"(v[2][0]), "+v"(v[2][1]), "+v"(v[2][2]), "+v"(v[2][3]) :: "memory");
    asm volatile("" : "+v"(v[3][0]), "+v"(v[3][1]), "+v"(v[3][2]), "+v"(v[3][3]), "+v"(v[4][0]), "+v"(v[4][1]), "+v"(v[4][2]),
                 "+v"(v[4][3]), "+v"(v[5][0]), "+v"(v[5][1]), "+v"(v[5][2]), "+v"(v[5][3]) :: "memory");
#pragma unroll
    for (int j = 0; j < 6; ++j) {
      int e = tid + 512 * j;
      f32x4 sm = (v[j][0] + v[j][1]) + (v[j][2] + v[j][3]);
      s16x4 o;
#pragma unroll
      for (int r = 0; r < 4; ++r) o[r] = f2bf(sm[r]);
      if (e < 2688) *(s16x4*)(out + (size_t)(mt * 96 + e / 28) * 896 + blk * 448 + nt * 112 + (e % 28) * 4) = o;
    }
  }
}

extern "C" void kernel_launch(void* const* d_in, const int* in_sizes, int n_in, void* d_out, int out_size, void* d_ws,
                              size_t ws_size, hipStream_t stream) {
  const u16* x0 = (const u16*)d_in[0];
  const u16* k0 = (const u16*)d_in[1];
  const u16* v0 = (const u16*)d_in[2];
  const u16* wq0 = (const u16*)d_in[3];
  const u16* wo0 = (const u16*)d_in[4];
  const u16* x1 = (const u16*)d_in[5];
  const u16* k1 = (const u16*)d_in[6];
  const u16* v1 = (const u16*)d_in[7];
  const u16* wq1 = (const u16*)d_in[8];
  const u16* wo1 = (const u16*)d_in[9];
  u16* O = (u16*)d_ws;
  float* part = (float*)((char*)d_ws + (size_t)2 * 768 * 4096 * 2);
  unsigned* cnt = (unsigned*)((char*)d_ws + (size_t)32 * 1024 * 1024);
  (void)hipFuncSetAttribute((const void*)k1_qattn, hipFuncAttributeMaxDynamicSharedMemorySize, K1_LDS);
  (void)hipFuncSetAttribute((const void*)k2_oproj, hipFuncAttributeMaxDynamicSharedMemorySize, K2_LDS);
  k1_qattn<<<256, 512, K1_LDS, stream>>>(x0, k0, v0, wq0, x1, k1, v1, wq1, O, cnt, wo0, wo1);
  k2_oproj<<<256, 512, K2_LDS, stream>>>(O, wo0, wo1, part, cnt, (u16*)d_out);
}
```
